```python
import torch

def _softmax(x, axis=-1):
    return torch.softmax(x, dim=axis)
def _gelu(x):
    return torch.nn.functional.gelu(x, approximate="tanh")
def _silu(x):
    return torch.nn.functional.silu(x)
def _sigmoid(x):
    return torch.sigmoid(x)
def _layernorm(x, axis=-1, eps=1e-5):
    m = x.mean(dim=axis, keepdim=True)
    v = x.var(dim=axis, keepdim=True, unbiased=False)
    return (x - m) / torch.sqrt(v + eps)
def _scatter_add(tgt, idx, src, axis=0):
    return tgt.clone().index_add_(axis, idx, src)
def _segment_sum(src, seg, n):
    out = torch.zeros((n,) + src.shape[1:], dtype=src.dtype)
    return out.index_add_(0, seg, src)
def _pad(x, pw):
    flat = tuple(v for pair in reversed(pw) for v in pair)
    return torch.nn.functional.pad(x, flat)

kernel_name = 'gated_mix__ssm_conv_s4__linear_attn_block__prompt'

def setup_inputs():

    _w = (lambda a: (lambda t: t.to(torch.bfloat16) if t.is_floating_point() else t)(torch.as_tensor(a)))
    def _A_setup():
        B, L, D, N = 1, 2048, 640, 128
        A_re = -(lambda _x: torch.exp(torch.as_tensor(_x)))((lambda *s: torch.randn(*s, dtype=torch.bfloat16) * 3.2)(D, N))
        A_im = torch.pi * (lambda *s: torch.randn(*s, dtype=torch.bfloat16) * 0.05)(D, N)
        B_re = (lambda *s: torch.randn(*s, dtype=torch.bfloat16) * 0.2)(D, N)
        B_im = (lambda *s: torch.randn(*s, dtype=torch.bfloat16) * 0.2)(D, N)
        C_re = (lambda *s: torch.randn(*s, dtype=torch.bfloat16) * 0.2)(D, N)
        C_im = (lambda *s: torch.randn(*s, dtype=torch.bfloat16) * 0.2)(D, N)
        log_dt = (lambda *s: torch.randn(*s, dtype=torch.bfloat16) * 0.05)(D) * 0.5 - 4.0
        Dskip = (lambda *s: torch.randn(*s, dtype=torch.bfloat16) * 0.05)(D)
        return {
            "u": _w((lambda *s: torch.randn(*s, dtype=torch.bfloat16) * 0.05)(B, L, D)),
            "A_re": _w(A_re),
            "A_im": _w(A_im),
            "B_re": _w(B_re),
            "B_im": _w(B_im),
            "C_re": _w(C_re),
            "C_im": _w(C_im),
            "log_dt": _w(log_dt),
            "Dskip": _w(Dskip),
        }
    _A_in = _A_setup()

    def _B_setup():
        B, S, D = 1, 2048, 640
        return {
            "x": _w((lambda *s: torch.randn(*s, dtype=torch.bfloat16) * 0.05)(B, S, D)),
            "wqkv": _w((lambda *s: torch.randn(*s, dtype=torch.bfloat16) * 0.8)(D, 3 * D)),
            "wo": _w((lambda *s: torch.randn(*s, dtype=torch.bfloat16) * 0.05)(D, D)),
        }
    _B_in = _B_setup()

    return {
        "A_u": _A_in["u"],
        "A_A_re": _A_in["A_re"],
        "A_A_im": _A_in["A_im"],
        "A_B_re": _A_in["B_re"],
        "A_B_im": _A_in["B_im"],
        "A_C_re": _A_in["C_re"],
        "A_C_im": _A_in["C_im"],
        "A_log_dt": _A_in["log_dt"],
        "A_Dskip": _A_in["Dskip"],
        "B_x": _B_in["x"],
        "B_wqkv": _B_in["wqkv"],
        "B_wo": _B_in["wo"],
        "gate_w": _w((lambda *s: torch.randn(*s, dtype=torch.bfloat16) * 0.05)(640) * 16.0),
    }

def reference(A_u, A_A_re, A_A_im, A_B_re, A_B_im, A_C_re, A_C_im, A_log_dt, A_Dskip, B_x, B_wqkv, B_wo, gate_w):
    def _A_ref(u, A_re, A_im, B_re, B_im, C_re, C_im, log_dt, Dskip):
        Bsz, L, D = u.shape
        dt = torch.exp(log_dt)[:, None]
        A = A_re + 1j * A_im
        Bc = B_re + 1j * B_im
        Cc = C_re + 1j * C_im
        logdA = dt * A
        dB = dt * Bc
        l = (lambda x, dt: x.to(dt))(torch.arange(L), A_re.dtype)
        P = torch.exp(logdA[:, :, None] * l[None, None, :])
        K = torch.einsum("dn,dnl,dn->dl", Cc, P, dB).real
        L2 = 2 * L
        Kp = _pad(K, ((0, 0), (0, L)))
        up = (lambda x, *d: x.permute(*d))(u, 0, 2, 1)
        up = _pad(up, ((0, 0), (0, 0), (0, L)))
        Kf = (lambda x, axes=(-2,-1): torch.fft.rfft2(x.float(), dim=axes))(Kp[:, None, :], axes=(-2, -1))
        Uf = (lambda x, axes=(-2,-1): torch.fft.rfft2(x.float(), dim=axes))(up[:, :, None, :], axes=(-2, -1))
        Yf = Uf * Kf[None, :, :, :]
        y = (lambda x, s, axes=(-2,-1): torch.fft.irfft2(x, s=s, dim=axes).to(torch.bfloat16))(Yf, (1, L2), axes=(-2, -1))[:, :, 0, :L]
        y = (lambda x, *d: x.permute(*d))(y, 0, 2, 1) + u * Dskip[None, None, :]
        return y

    def _B_ref(x, wqkv, wo):
        B, S, D = x.shape
        H = 32; Dh = D // H
        qkv = (lambda x, *d: x.permute(*d))((x @ wqkv).reshape(B, S, 3, H, Dh), 2, 0, 3, 1, 4)
        q, k, v = qkv[0], qkv[1], qkv[2]
        phi = lambda t: torch.where(t > 0, t + 1, torch.exp(t)) + 1e-6
        pq, pk = phi(q), phi(k)
        kv = torch.einsum("bhsd,bhse->bhde", pk, v)
        z = torch.einsum("bhsd->bhd", pk)
        num = torch.einsum("bhqd,bhde->bhqe", pq, kv)
        den = torch.einsum("bhqd,bhd->bhq", pq, z)[..., None] + 1e-6
        o = num / den
        return (lambda x, *d: x.permute(*d))(o, 0, 2, 1, 3).reshape(B, S, D) @ wo

    x = A_u
    g = _sigmoid((x * gate_w).sum(-1, keepdims=True))
    a = _A_ref(x, A_A_re, A_A_im, A_B_re, A_B_im, A_C_re, A_C_im, A_log_dt, A_Dskip)
    b = _B_ref(x, B_wqkv, B_wo)
    return g * a + (1 - g) * b
```

```cpp
#include <hip/hip_runtime.h>
#include <hip/hip_bf16.h>
#include <stdint.h>

#if defined(__HIP_DEVICE_COMPILE__)
#pragma clang attribute push(__attribute__((target("no-packed-fp32-ops"))), apply_to = function)
#endif
typedef unsigned short u16;
typedef short bf16x8 __attribute__((ext_vector_type(8)));
typedef short s16x4 __attribute__((ext_vector_type(4)));
typedef float f32x2 __attribute__((ext_vector_type(2)));
typedef float f32x4 __attribute__((ext_vector_type(4)));
typedef float f32x16 __attribute__((ext_vector_type(16)));
typedef unsigned u32x2 __attribute__((ext_vector_type(2)));
typedef unsigned u32x4 __attribute__((ext_vector_type(4)));
typedef __bf16 bf16x2_t __attribute__((ext_vector_type(2)));
#define LAS __attribute__((address_space(3)))
#define DI __device__ __forceinline__

constexpr int SL = 2048, DM = 640, NS = 128;
constexpr int KSR_BYTES = 2176;

constexpr size_t WS_UT   = 0;
constexpr size_t WS_KSR  = WS_UT + (size_t)DM * SL * 2;
constexpr size_t WS_PQF  = WS_KSR + (size_t)DM * KSR_BYTES;
constexpr size_t WS_PART = WS_PQF + (size_t)SL * DM * 2;
constexpr size_t WS_G    = WS_PART + (size_t)16 * 32 * 420 * 4;
constexpr size_t WS_MTF  = WS_G + (size_t)10 * SL * 4;
constexpr size_t WS_AT   = WS_MTF + (size_t)DM * DM * 2;
constexpr size_t WS_PQN  = WS_PQF;
constexpr size_t WS_END  = WS_AT + (size_t)DM * SL * 2;
constexpr int HO_CHUNKS = (int)((WS_END - WS_MTF) / 16);

DI unsigned pk2(float a, float b) { f32x2 v = {a, b}; return __builtin_bit_cast(unsigned, __builtin_convertvector(v, bf16x2_t)); }
DI float lo2f(unsigned u) { return __builtin_bit_cast(float, u << 16); }
DI float hi2f(unsigned u) { return __builtin_bit_cast(float, u & 0xffff0000u); }
DI float rbf(float x) { return lo2f(pk2(x, x)); }
DI f32x4 mfma16(u32x4 a, u32x4 b, f32x4 c) {
  return __builtin_amdgcn_mfma_f32_16x16x32_bf16(__builtin_bit_cast(bf16x8, a), __builtin_bit_cast(bf16x8, b), c, 0, 0, 0);
}
DI u32x2 trread(LAS char* p) { return __builtin_bit_cast(u32x2, __builtin_amdgcn_ds_read_tr16_b64_v4i16((LAS s16x4*)p)); }
DI void bsync() {
  __builtin_amdgcn_fence(__ATOMIC_RELEASE, "workgroup");
  __builtin_amdgcn_s_barrier();
  __builtin_amdgcn_fence(__ATOMIC_ACQUIRE, "workgroup");
}
#define CFENCE() asm volatile("" ::: "memory")
DI float phi(float t) { return __builtin_amdgcn_fmed3f(t, 1e-6f, __builtin_inff()) + __builtin_amdgcn_fmed3f(__builtin_amdgcn_exp2f(t * 1.4426950408889634f), 0.f, 1.f); }
constexpr int AUX_SC1 = 16;
DI __amdgpu_buffer_rsrc_t ws_rsrc(char* ws) { return __builtin_amdgcn_make_buffer_rsrc((void*)ws, 0, (int)WS_END, 0x00020000); }
DI u32x4 ld128s(__amdgpu_buffer_rsrc_t rs, int off) { return __builtin_amdgcn_raw_buffer_load_b128(rs, off, 0, AUX_SC1); }
DI u32x4 ld128u(__amdgpu_buffer_rsrc_t rs, int voff, int soff) { return __builtin_amdgcn_raw_buffer_load_b128(rs, voff, soff, AUX_SC1); }
DI void st128s(__amdgpu_buffer_rsrc_t rs, int off, u32x4 v) { __builtin_amdgcn_raw_buffer_store_b128(v, rs, off, 0, AUX_SC1); }
DI void st64s(__amdgpu_buffer_rsrc_t rs, int off, u32x2 v) { __builtin_amdgcn_raw_buffer_store_b64(v, rs, off, 0, AUX_SC1); }
DI void k1st128(char* ws, size_t off, u32x4 v) { __builtin_amdgcn_raw_buffer_store_b128(v, ws_rsrc(ws), (int)off, 0, AUX_SC1); }
DI void k1st64(char* ws, size_t off, u32x2 v) { __builtin_amdgcn_raw_buffer_store_b64(v, ws_rsrc(ws), (int)off, 0, AUX_SC1); }
DI unsigned umax3(unsigned a, unsigned b, unsigned c) { return max(max(a, b), c); }
DI unsigned umax4(u32x4 v, unsigned m) { return umax3(umax3(v.x, v.y, m), v.z, v.w); }
DI unsigned and4(u32x4 v, unsigned m) { return (v.x & v.y) & (v.z & v.w) & m; }
DI bool all_neg(unsigned m) { return __builtin_amdgcn_ballot_w64((int)m >= 0) == 0ull; }
DI bool all_written(unsigned m) { return __builtin_amdgcn_ballot_w64(m == 0xffffffffu) == 0ull; }
constexpr int SPIN_CAP = 1 << 14;
DI void glds16(unsigned voff, const void* sbase, unsigned lds_dst) {
  asm volatile("s_mov_b32 m0, %2\n\ts_nop 0\n\tglobal_load_lds_dwordx4 %0, %1" :: "v"(voff), "s"(sbase), "s"(lds_dst) : "memory");
}
template <int IMM> DI void gload16(u32x4& dst, unsigned voff, const void* sbase) {
  asm volatile("global_load_dwordx4 %0, %1, %2 offset:%3" : "=v"(dst) : "v"(voff), "s"(sbase), "i"(IMM) : "memory");
}
constexpr int XAREA = 16384, SLOT = XAREA + 20480, NSLOT = 3, K1_EXTRA = NSLOT * SLOT, K1_SIDE = K1_EXTRA + 4 * 4352, K1_CNT = K1_SIDE + 16384, K1_LDS = K1_CNT + 16;
template <int C> DI float dpp_add(float v) {
  return v + __builtin_bit_cast(float, __builtin_amdgcn_update_dpp(0, __builtin_bit_cast(int, v), C, 0xf, 0xf, false));
}
DI void lbar() { asm volatile("s_waitcnt lgkmcnt(0)\n\ts_barrier" ::: "memory"); }
DI void role_loader(int rt, int c0, int c1, int bs, int wr, const u16* __restrict__ x, const u16* __restrict__ wqkv, const u16* __restrict__ gw,
                    char* ws, LAS char* sm) {
  const int lane = threadIdx.x & 63;
  const unsigned smb = (unsigned)(uintptr_t)sm;
  const unsigned wdst = smb + XAREA + 640u * wr, xdst = smb + 4096u * wr;
  const unsigned xoff = (unsigned)((128 * rt + 32 * wr + (lane >> 3)) * DM + 8 * ((lane & 7) ^ (lane >> 3))) * 2u;
  unsigned woff;
  {
    const int r = lane >= 20, ch = lane - 20 * r, hf = (ch >= 10) ^ (wr >> 1);
    woff = (unsigned)(((wr >> 1) * 8 + 2 * (wr & 1) + r) * 1920 + (hf ? c1 : c0) + 8 * (ch >= 10 ? ch - 10 : ch)) * 2u;
  }
  auto issue = [&](int P, int sl, int i0, int i1) {
    const char* wbase = (const char*)wqkv + (size_t)P * 64 * 1920 * 2;
    const char* xbase = (const char*)x + P * 128;
    const unsigned so = sl * SLOT;
    if (lane < 40) {
#pragma unroll
      for (int i = 2 * i0; i < 2 * i1; ++i) glds16(woff, wbase + (4 * (i & 1) + 16 * (i >> 1)) * 1920 * 2, wdst + so + 2560u * i);
    }
#pragma unroll
    for (int i = i0; i < i1; ++i) glds16(xoff, xbase + i * 8 * DM * 2, xdst + so + 1024u * i);
  };
  issue(0, 0, 0, 4);
  issue(1, 1, 0, 2);
  asm volatile("s_waitcnt vmcnt(6)\n\ts_barrier" ::: "memory");
  issue(1, 1, 2, 4);
  int sl = 2;
#pragma unroll 1
  for (int P = 2; P < 9; ++P) {
    issue(P, sl, 0, 4);
    sl = sl == 2 ? 0 : sl + 1;
    asm volatile("s_waitcnt vmcnt(12)\n\ts_barrier" ::: "memory");
  }
  issue(9, 0, 0, 4);
#pragma unroll
  for (int i = 0; i < 4; ++i) glds16(xoff, (const char*)x + min(bs, 9) * 128 + i * 8 * DM * 2, xdst + K1_SIDE + 1024u * i);
  u32x4 gv;
  gload16<0>(gv, (unsigned)(64 * min(bs, 9) + 8 * (lane & 7)) * 2u, gw);
  asm volatile("s_waitcnt vmcnt(17)\n\ts_barrier\n\ts_waitcnt vmcnt(1)\n\ts_barrier\n\ts_waitcnt vmcnt(0)" : "+v"(gv) :: "memory");
  if (bs < 10) {
    const int c16 = lane & 15, g = lane >> 4, q4 = c16 >> 2, p4 = lane & 3;
    LAS char* xs = sm + K1_SIDE;
    LAS char* bb = sm + K1_EXTRA + wr * 4352;
#pragma unroll
    for (int j = 0; j < 4; ++j) {
      const int t = 32 * g + 8 * j + q4;
      const u32x2 r0 = trread(xs + t * 128 + (((2 * wr + (p4 >> 1)) ^ (t & 7)) << 4) + (p4 & 1) * 8);
      const u32x2 r1 = trread(xs + (t + 4) * 128 + (((2 * wr + (p4 >> 1)) ^ ((t + 4) & 7)) << 4) + (p4 & 1) * 8);
      *(LAS u32x4*)(bb + c16 * 272 + g * 64 + j * 16) = u32x4{r0.x, r0.y, r1.x, r1.y};
    }
    CFENCE();
#pragma unroll
    for (int i = 0; i < 4; ++i)
      k1st128(ws, WS_UT + ((size_t)(64 * bs + 16 * wr + 4 * i + g) * SL + 128 * rt) * 2 + c16 * 16, *(LAS u32x4*)(bb + (4 * i + g) * 272 + c16 * 16));
    float gsel = 0.f;
#pragma unroll
    for (int i = 0; i < 4; ++i) {
      const u32x4 xv = *(LAS u32x4*)(xs + (32 * wr + 8 * i + (lane >> 3)) * 128 + (((lane & 7) ^ (lane >> 3)) << 4));
      float v = 0.f;
#pragma unroll
      for (int e = 0; e < 4; ++e) {
        const unsigned pr = pk2(lo2f(xv[e]) * lo2f(gv[e]), hi2f(xv[e]) * hi2f(gv[e]));
        v += lo2f(pr) + hi2f(pr);
      }
      v = dpp_add<0x141>(dpp_add<0x4e>(dpp_add<0xb1>(v)));
      gsel = (lane & 7) == i ? v : gsel;
    }
    if ((lane & 7) < 4) ((float*)(ws + WS_G))[bs * SL + 128 * rt + 32 * wr + 8 * (lane & 7) + (lane >> 3)] = gsel;
  }
}

template <bool TRANSPOSED> DI void gemm_loop(LAS char* sm, int wr, int o0, int o1, int o2, f32x4 (&acc)[2][5]) {
  constexpr int ts = TRANSPOSED ? 32 : 40;
  const int lane = threadIdx.x & 63, c16 = lane & 15, g = lane >> 4;
  const int x0 = (32 * wr + c16) * 128 + ((g ^ (c16 & 7)) << 4), x1 = (32 * wr + c16) * 128 + (((4 + g) ^ (c16 & 7)) << 4);
  u32x4 xf[2][2], wf[2][5];
#pragma unroll
  for (int m = 0; m < 2; ++m) {
    xf[1][m] = u32x4{0u, 0u, 0u, 0u};
#pragma unroll
    for (int t = 0; t < 5; ++t) acc[m][t] = f32x4{0.f, 0.f, 0.f, 0.f};
  }
#pragma unroll
  for (int t = 0; t < 5; ++t) wf[1][t] = u32x4{0u, 0u, 0u, 0u};
  auto mm = [&](int b) {
#pragma unroll
    for (int t = 0; t < 5; ++t)
#pragma unroll
      for (int m = 0; m < 2; ++m) acc[m][t] = TRANSPOSED ? mfma16(wf[b][t], xf[b][m], acc[m][t]) : mfma16(xf[b][m], wf[b][t], acc[m][t]);
  };
  int so = 0;
#pragma unroll 1
  for (int P = 0; P < 10; ++P) {
    LAS char* sb = sm + so;
    so = so == 2 * SLOT ? 0 : so + SLOT;
    lbar();
#pragma unroll
    for (int kk = 0; kk < 2; ++kk) {
      __builtin_amdgcn_sched_barrier(0);
      LAS char* xr = sb + (kk ? x1 : x0);
      xf[kk][0] = *(LAS u32x4*)(xr); xf[kk][1] = *(LAS u32x4*)(xr + 2048);
#pragma unroll
      for (int t = 0; t < 5; ++t) {
        LAS char* p = sb + (t < 2 ? o0 + ts * t : (t < 4 ? o1 + ts * (t - 2) : o2)) + kk * 32 * 320;
        const u32x2 lo = trread(p), hi = trread(p + 8 * 320);
        wf[kk][t] = u32x4{lo.x, lo.y, hi.x, hi.y};
      }
      mm(kk ^ 1);
#pragma unroll
      for (int i = 0; i < 10; ++i) { __builtin_amdgcn_sched_group_barrier(0x008, 1, 0); __builtin_amdgcn_sched_group_barrier(0x100, 1, 0); if (i < 2) __builtin_amdgcn_sched_group_barrier(0x100, 1, 0); }
    }
    __builtin_amdgcn_sched_barrier(0);
  }
  mm(1);
}

DI void role_kv(int rt, int hq, char* ws, LAS char* sm) {
  const int tid = threadIdx.x, lane = tid & 63, w = __builtin_amdgcn_readfirstlane(tid >> 6), wr = w & 3, hb = w >> 2;
  const int c16 = lane & 15, g = lane >> 4, q4 = c16 >> 2, p4 = lane & 3;
  const int rowpart = XAREA + (16 * (g >> 1) + 4 * (g & 1) + q4) * 320 + hb * 80;
  LAS char* ro[6];
  {
    LAS char* rb = sm + SLOT + w * 4096;
    const int ea = c16 == 0 ? 20 : (q4 == 2 ? 16 + p4 : -1), eb = c16 == 0 ? 20 : (q4 == 3 ? 16 + p4 : -1);
    ro[0] = rb + (c16 * 5 + g) * 16;
    ro[1] = rb + (ea >= 0 ? ea * 5 + g : 255) * 16;
    ro[2] = rb + (eb >= 0 ? 105 + eb * 5 + g : 255) * 16;
    ro[3] = rb + (g == 0 ? c16 * 5 + 4 : 255) * 16;
    ro[4] = rb + (g == 1 ? 105 + c16 * 5 + 4 : 255) * 16;
    ro[5] = rb + (g == 0 && ea >= 0 ? ea * 5 + 4 : (g == 1 && eb >= 0 ? 105 + eb * 5 + 4 : 255)) * 16;
#pragma unroll
    for (int i = 0; i < 6; ++i) asm volatile("" : "+v"(ro[i]));
  }
  if (tid < 2) ((LAS unsigned*)(sm + K1_CNT))[tid] = 0u;
  f32x4 acc[2][5];
  gemm_loop<false>(sm, wr, rowpart + (g & 1) * 160 + 8 * p4, rowpart + ((g & 1) ^ 1) * 160 + 8 * p4,
                   rowpart + ((g & 1) ^ (p4 >> 1)) * 160 + 32 + 40 * (p4 & 1), acc);
  u32x4 F[4], F4A, F4B;
#pragma unroll
  for (int t = 0; t < 2; ++t)
    F[t] = u32x4{pk2(phi(acc[0][t][0]), phi(acc[0][t][1])), pk2(phi(acc[0][t][2]), phi(acc[0][t][3])),
                 pk2(phi(acc[1][t][0]), phi(acc[1][t][1])), pk2(phi(acc[1][t][2]), phi(acc[1][t][3]))};
#pragma unroll
  for (int t = 2; t < 4; ++t)
    F[t] = u32x4{pk2(acc[0][t][0], acc[0][t][1]), pk2(acc[0][t][2], acc[0][t][3]), pk2(acc[1][t][0], acc[1][t][1]), pk2(acc[1][t][2], acc[1][t][3])};
  F4A = u32x4{pk2(phi(acc[0][4][0]), phi(acc[0][4][1])), pk2(phi(acc[0][4][2]), phi(acc[0][4][3])),
              pk2(phi(acc[1][4][0]), phi(acc[1][4][1])), pk2(phi(acc[1][4][2]), phi(acc[1][4][3]))};
  {
    const u32x4 v = u32x4{pk2(acc[0][4][0], acc[0][4][1]), pk2(acc[0][4][2], acc[0][4][3]), pk2(acc[1][4][0], acc[1][4][1]), pk2(acc[1][4][2], acc[1][4][3])};
    const bool one = c16 < 8;
    F4B = u32x4{one ? 0x3f803f80u : v.x, one ? 0x3f803f80u : v.y, one ? 0x3f803f80u : v.z, one ? 0x3f803f80u : v.w};
  }
  const f32x4 z4 = f32x4{0.f, 0.f, 0.f, 0.f};
  {
    const f32x4 P0 = mfma16(F[0], F[2], z4), P1 = mfma16(F[0], F4B, z4), P2 = mfma16(F4A, F[2], z4), P3 = mfma16(F4A, F4B, z4);
    const f32x4 P4 = mfma16(F[1], F[3], z4), P5 = mfma16(F[1], F4B, z4), P6 = mfma16(F4A, F[3], z4);
    *(LAS f32x4*)(ro[0]) = P0; *(LAS f32x4*)(ro[0] + 1680) = P4; *(LAS f32x4*)(ro[1]) = P1; *(LAS f32x4*)(ro[2]) = P5;
    *(LAS f32x4*)(ro[3]) = P2; *(LAS f32x4*)(ro[4]) = P6; *(LAS f32x4*)(ro[5]) = P3;
  }
  {
    LAS unsigned* cnt = (LAS unsigned*)(sm + K1_CNT) + hb;
    asm volatile("s_waitcnt lgkmcnt(0)" ::: "memory");
    if (lane == 0) __hip_atomic_fetch_add(cnt, 1u, __ATOMIC_RELAXED, __HIP_MEMORY_SCOPE_WORKGROUP);
    while (__hip_atomic_load(cnt, __ATOMIC_RELAXED, __HIP_MEMORY_SCOPE_WORKGROUP) < 4u) __builtin_amdgcn_s_sleep(1);
    CFENCE();
  }
  if ((tid & 255) < 210) {
    LAS char* rb = sm + SLOT + hb * 16384 + (tid & 255) * 16;
    const f32x4 s = (*(LAS f32x4*)(rb) + *(LAS f32x4*)(rb + 4096)) + (*(LAS f32x4*)(rb + 8192) + *(LAS f32x4*)(rb + 12288));
    const int f = tid & 255, hh = f >= 105;
    k1st128(ws, WS_PART + ((size_t)((4 * hq + 2 * hb + hh) * 16 + rt) * 105 + f - 105 * hh) * 16, __builtin_bit_cast(u32x4, s));
  }
}

DI void role_q(int rt, int qc, char* ws, LAS char* sm) {
  const int tid = threadIdx.x, lane = tid & 63, w = __builtin_amdgcn_readfirstlane(tid >> 6), wr = w & 3, ch = w >> 2;
  const int c16 = lane & 15, g = lane >> 4, q4 = c16 >> 2, p4 = lane & 3;
  const int o = XAREA + (16 * (g >> 1) + 4 * (g & 1) + q4) * 320 + (ch ^ (g & 1)) * 160 + 8 * p4;
  f32x4 acc[2][5];
  gemm_loop<true>(sm, wr, o + 32 * ch, o + 32 * ch + 64, o + (ch ? 0 : 128), acc);
#pragma unroll
  for (int m = 0; m < 2; ++m) {
    const size_t fb = WS_PQF + (size_t)((8 * rt + 2 * wr + m) * 20 + 5 * qc) * 1024;
    unsigned v[5][2];
#pragma unroll
    for (int t = 0; t < 5; ++t) {
      const unsigned q01 = pk2(acc[m][t][0], acc[m][t][1]), q23 = pk2(acc[m][t][2], acc[m][t][3]);
      v[t][0] = pk2(phi(lo2f(q01)), phi(hi2f(q01))); v[t][1] = pk2(phi(lo2f(q23)), phi(hi2f(q23)));
    }
#pragma unroll
    for (int u = 0; u < 2; ++u) {
#pragma unroll
      for (int z = 0; z < 2; ++z) {
        const auto r = __builtin_amdgcn_permlane32_swap(v[2 * u][z], v[2 * u + 1][z], false, false);
        const auto q = __builtin_amdgcn_permlane16_swap(r[0], r[1], false, false);
        v[2 * u][z] = q[0]; v[2 * u + 1][z] = q[1];
      }
      k1st128(ws, fb + (3 * ch + u) * 1024 + lane * 16, u32x4{v[2 * u][0], v[2 * u][1], v[2 * u + 1][0], v[2 * u + 1][1]});
    }
    k1st64(ws, fb + 2048 + ((2 * ch + (g >> 1)) * 16 + c16) * 16 + (g & 1) * 8, u32x2{v[4][0], v[4][1]});
  }
}

struct cf { float r, i; };
DI cf cmul(cf a, cf b) { return cf{a.r * b.r - a.i * b.i, a.r * b.i + a.i * b.r}; }
DI cf csq(cf a) { return cf{a.r * a.r - a.i * a.i, 2.f * a.r * a.i}; }
DI cf cexp_m(float m, float a2, float wh, float wl) {
  const float e = __builtin_amdgcn_exp2f(m * a2);
  const float ph = __builtin_amdgcn_fractf(m * wh) + m * wl;
  return cf{e * __builtin_amdgcn_cosf(ph), e * __builtin_amdgcn_sinf(ph)};
}
constexpr int KG_RS = 272, KG_Y = 21 * KG_RS, KG_T = 0, KG_WAVE = KG_Y + 32 * KG_RS;

typedef _Float16 h16x2 __attribute__((ext_vector_type(2)));
typedef _Float16 h16x8 __attribute__((ext_vector_type(8)));
DI unsigned pkh(float a, float b) { f32x2 v = {a, b}; return __builtin_bit_cast(unsigned, __builtin_convertvector(v, h16x2)); }
DI unsigned hmul(unsigned a, unsigned b) {
  unsigned t, r;
  asm("v_pk_mul_f16 %0, %1, %2 op_sel:[0,0] op_sel_hi:[0,1]" : "=v"(t) : "v"(a), "v"(b));
  asm("v_pk_fma_f16 %0, %1, %2, %3 op_sel:[1,1,0] op_sel_hi:[1,0,1] neg_lo:[0,1,0]" : "=v"(r) : "v"(a), "v"(b), "v"(t));
  return r;
}
template <int C> DI unsigned dpp_umax(unsigned v) { return max(v, (unsigned)__builtin_amdgcn_update_dpp(0, (int)v, C, 0xf, 0xf, false)); }

DI void role_kgen(int d, unsigned pAre, unsigned pAim, unsigned pBre, unsigned pBim, unsigned pCre, unsigned pCim, unsigned ldw, unsigned dskw,
                  char* ws, LAS char* sm, u32x4& oD, u32x4& oR, u32x2& oX) {
  const int lane = threadIdx.x & 63;
  const float dt = rbf(expf((d & 1) ? hi2f(ldw) : lo2f(ldw)));
  const cf W0 = cmul(cf{lo2f(pCre), lo2f(pCim)}, cf{dt * lo2f(pBre), dt * lo2f(pBim)});
  const cf W1 = cmul(cf{hi2f(pCre), hi2f(pCim)}, cf{dt * hi2f(pBre), dt * hi2f(pBim)});
  unsigned mx = max(max(__builtin_bit_cast(unsigned, W0.r) & 0x7fffffffu, __builtin_bit_cast(unsigned, W0.i) & 0x7fffffffu),
                    max(__builtin_bit_cast(unsigned, W1.r) & 0x7fffffffu, __builtin_bit_cast(unsigned, W1.i) & 0x7fffffffu));
  mx = dpp_umax<0x121>(dpp_umax<0x122>(dpp_umax<0x124>(dpp_umax<0x128>(mx))));
  const unsigned mxs = max(max((unsigned)__builtin_amdgcn_readlane((int)mx, 0), (unsigned)__builtin_amdgcn_readlane((int)mx, 16)),
                           max((unsigned)__builtin_amdgcn_readlane((int)mx, 32), (unsigned)__builtin_amdgcn_readlane((int)mx, 48)));
  const unsigned ex = min(max(mxs >> 23, 1u), 253u);
  const float wsc = __builtin_bit_cast(float, (254u - ex) << 23), wun = __builtin_bit_cast(float, ex << 23);
  const unsigned Wh0 = pkh(W0.r * wsc, W0.i * wsc), Wh1 = pkh(W1.r * wsc, W1.i * wsc);

  f32x16 acc;
#pragma unroll
  for (int i = 0; i < 16; ++i) acc[i] = 0.f;
  const int rho = lane & 31, h = lane >> 5;
  LAS char* xa = sm + (rho < 21 ? rho : 20) * KG_RS + 16 * h;
  LAS char* ya = sm + KG_Y + rho * KG_RS + 16 * h;
#pragma unroll 1
  for (int p = 0; p < 2; ++p) {
    const int sh = p ? 0 : 16;
    const float lr = dt * hi2f(pAre << sh), li = dt * hi2f(pAim << sh);
    const float a2 = lr * 1.4426950408889634f;
    const float wh = li * 0.15915494f;
    const float wl = fmaf(li, 0.15915494f, -wh) + li * 6.4206383e-9f;
    const cf E1 = cexp_m(1.f, a2, wh, wl), E8 = cexp_m(8.f, a2, wh, wl), E64 = cexp_m(64.f, a2, wh, wl), E512 = cexp_m(512.f, a2, wh, wl);
    const cf E2 = csq(E1), E4 = csq(E2), E16 = csq(E8), E32 = csq(E16), E128 = csq(E64), E256 = csq(E128), E1024 = csq(E512);
    LAS char* xw = sm + 4 * lane;
    LAS char* yw = sm + KG_Y + 4 * lane;
    auto putx = [&](int row, unsigned v) { *(LAS unsigned*)(xw + row * KG_RS) = v; };
    auto puty = [&](int row, unsigned v) { *(LAS unsigned*)(yw + row * KG_RS) = v; };
    {
      const unsigned e1 = pkh(E1.r, E1.i), e2 = pkh(E2.r, E2.i), e4 = pkh(E4.r, E4.i), e256 = pkh(E256.r, E256.i), e512 = pkh(E512.r, E512.i),
                     e1024 = pkh(E1024.r, E1024.i);
      unsigned X[8];
      X[0] = p ? Wh1 : Wh0; X[1] = hmul(X[0], e1); X[2] = hmul(X[0], e2); X[3] = hmul(X[1], e2);
#pragma unroll
      for (int i = 0; i < 4; ++i) X[4 + i] = hmul(X[i], e4);
#pragma unroll
      for (int i = 0; i < 8; ++i) putx(i, X[i]);
      const unsigned a0 = hmul(X[0], e256), a2_ = hmul(X[2], e256), a4 = hmul(X[4], e256), a6 = hmul(X[6], e256);
      putx(8, a0); putx(9, a2_); putx(10, a4); putx(11, a6);
      const unsigned b0 = hmul(X[0], e512), b4 = hmul(X[4], e512), c0 = hmul(a0, e512), c4 = hmul(a4, e512);
      putx(12, b0); putx(13, b4); putx(14, c0); putx(15, c4);
      const unsigned d0 = hmul(X[0], e1024);
      putx(16, d0); putx(17, hmul(a0, e1024)); putx(18, hmul(b0, e1024)); putx(19, hmul(c0, e1024));
      putx(20, hmul(d0, e1024));
    }
    {
      unsigned Y[16];
      const unsigned e128 = pkh(E128.r, -E128.i);
      Y[0] = 0x3c00u; Y[1] = pkh(E8.r, -E8.i); Y[2] = pkh(E16.r, -E16.i); Y[4] = pkh(E32.r, -E32.i); Y[8] = pkh(E64.r, -E64.i);
      Y[3] = hmul(Y[1], Y[2]);
#pragma unroll
      for (int i = 1; i < 4; ++i) Y[4 + i] = hmul(Y[i], Y[4]);
#pragma unroll
      for (int i = 1; i < 8; ++i) Y[8 + i] = hmul(Y[i], Y[8]);
#pragma unroll
      for (int i = 0; i < 16; ++i) puty(i, Y[i]);
      puty(16, e128);
#pragma unroll
      for (int i = 1; i < 16; ++i) puty(16 + i, hmul(Y[i], e128));
    }
    CFENCE();

#pragma unroll
    for (int ks = 0; ks < 8; ++ks) {
      const u32x4 a = *(LAS u32x4*)(xa + 32 * ks), b = *(LAS u32x4*)(ya + 32 * ks);
      acc = __builtin_amdgcn_mfma_f32_32x32x16_f16(__builtin_bit_cast(h16x8, a), __builtin_bit_cast(h16x8, b), acc, 0, 0, 0);
    }
    CFENCE();
  }
#pragma unroll
  for (int i = 0; i < 12; ++i) acc[i] *= wun;

  LAS char* T = sm + KG_T;
  const int b = rho;
  *(LAS u32x2*)(T + (8 * b + 4 * h) * 2) = u32x2{pk2(acc[0], acc[1]), pk2(acc[2], acc[3])};
  {
    const int i0 = h ? 384 + 2 * b : 256 + 4 * b, i1 = h ? 448 + 2 * b : 258 + 4 * b;
    *(LAS unsigned*)(T + i0 * 2) = pk2(acc[4], acc[5]);
    *(LAS unsigned*)(T + i1 * 2) = pk2(acc[6], acc[7]);
  }
  if (h == 0) {
#pragma unroll
    for (int a = 0; a < 4; ++a) *(LAS u16*)(T + (512 + 32 * a + b) * 2) = (u16)pk2(acc[8 + a], 0.f);
  } else if (b == 0) {
    *(LAS u16*)(T + 640 * 2) = (u16)pk2(acc[8], 0.f);
  }
  CFENCE();
  auto tb = [&](int idx) -> unsigned { return *(LAS u16*)(T + idx * 2); };
  {
    const u32x2 t = *(LAS u32x2*)(T + (384 + 4 * lane) * 2);
    oD = u32x4{(t.x & 0xffffu) * 0x10001u, (t.x >> 16) * 0x10001u, (t.y & 0xffffu) * 0x10001u, (t.y >> 16) * 0x10001u};
    oX = u32x2{tb(640) * 0x10001u, (d & 1) ? dskw & 0xffff0000u : dskw << 16};
  }
  {
    const int m = 63 - lane;
    const bool up = m >= 32;
    const int i0 = up ? 128 + 4 * m : 8 * m;
    unsigned v[8];
#pragma unroll
    for (int e = 0; e < 8; ++e) v[e] = tb(i0 + e);
    const u32x4 A = u32x4{v[4] | (v[3] << 16), v[2] * 0x10001u, v[2] | (v[1] << 16), v[0] * 0x10001u};
    const u32x4 B = u32x4{v[7] | (v[6] << 16), v[5] | (v[4] << 16), v[3] | (v[2] << 16), v[1] | (v[0] << 16)};
    oR = up ? A : B;
  }
}

__global__ void __launch_bounds__(768, 3) k1(const u16* x, const u16* Are, const u16* Aim, const u16* Bre, const u16* Bim, const u16* Cre,
                                             const u16* Cim, const u16* logdt, const u16* Dskip, const u16* wqkv, const u16* wo, const u16* gw, char* ws) {
  __shared__ __attribute__((aligned(16))) char smem[K1_LDS];
  LAS char* sm = (LAS char*)smem;
  asm volatile("gm_code_begin:");
  const int bid = blockIdx.x, xcd = bid & 7, j = bid >> 3;
  const int w = __builtin_amdgcn_readfirstlane(threadIdx.x >> 6), lane = threadIdx.x & 63;
  const bool aux = j >= 24, isq = j >= 16;
  const int rt = (xcd >> 1) * 4 + (j & 3);
  const int cq = !isq ? (xcd & 1) * 4 + (j >> 2) : (xcd & 1) * 2 + ((j >> 2) & 1);
  if (__builtin_expect(!aux && w >= 8, 1)) {
    const int bs = isq ? cq : cq + 4;
    role_loader(rt, isq ? 160 * cq : 640 + 80 * cq, isq ? 160 * cq + 80 : 1280 + 80 * cq, bs, w - 8, x, wqkv, gw, ws, sm);
    return;
  }
  if (!aux) {
    if (!isq) role_kv(rt, cq, ws, sm);
    else role_q(rt, cq, ws, sm);
  } else if (w < 10) {
    const int d = (bid - 192) * 10 + w, n2 = d * (NS / 2) + lane;
    const unsigned p0 = ((const unsigned*)Are)[n2], p1 = ((const unsigned*)Aim)[n2], p2 = ((const unsigned*)Bre)[n2], p3 = ((const unsigned*)Bim)[n2],
                   p4 = ((const unsigned*)Cre)[n2], p5 = ((const unsigned*)Cim)[n2], p6 = ((const unsigned*)logdt)[d >> 1], p7 = ((const unsigned*)Dskip)[d >> 1];
    asm volatile("s_barrier" ::: "memory");
    u32x4 oD, oR; u32x2 oX;
    role_kgen(d, p0, p1, p2, p3, p4, p5, p6, p7, ws, sm + w * KG_WAVE, oD, oR, oX);
    const size_t dst = WS_KSR + (size_t)d * KSR_BYTES;
    k1st128(ws, dst + lane * 16, oR); k1st128(ws, dst + 1024 + lane * 16, oD);
    if (lane == 0) k1st64(ws, dst + 2048, oX);
    for (int i = d * 64 + lane; i < HO_CHUNKS; i += 640 * 64) k1st128(ws, WS_MTF + (size_t)i * 16, u32x4{~0u, ~0u, ~0u, ~0u});
  } else if (w == 10) {
    const char* p[3]; int step[3];
#pragma unroll
    for (int i = 0; i < 3; ++i) {
      const int pw = min((j - 24) * 3 + i, 22);
      if (pw < 8) { p[i] = (const char*)x + (size_t)((xcd >> 1) * 512 + 64 * pw + lane) * (DM * 2); step[i] = 128; }
      else {
        const int idx = (pw - 8) * 64 + lane, row = idx / 15, l = idx - 15 * row, part = l / 5;
        p[i] = (const char*)wqkv + (size_t)row * 3840 + 1280 * part + 128 * (l - 5 * part) + 640 * (xcd & 1); step[i] = 64 * 3840;
      }
    }
    asm volatile("s_barrier" ::: "memory");
    unsigned v[24];
#pragma unroll
    for (int P = 2; P < 10; ++P)
#pragma unroll
      for (int i = 0; i < 3; ++i) v[(P - 2) * 3 + i] = *(const unsigned*)(p[i] + (size_t)P * step[i]);
#pragma unroll
    for (int i = 0; i < 24; ++i) asm volatile("" :: "v"(v[i]));
  } else {
    const char* cp;
    {
      unsigned long long pc; int lo, hi;
      asm volatile("s_getpc_b64 %0\ngm_code_ref:\n\ts_mov_b32 %1, gm_code_begin-gm_code_ref\n\ts_mov_b32 %2, gm_code_end-gm_code_ref" : "=s"(pc), "=s"(lo), "=s"(hi));
      const int o = lo + ((j - 24) * 64 + lane) * 128;
      const int oc = min(o, hi - 4);
      const unsigned v = *(const unsigned*)(((const char*)pc + oc) - (((uintptr_t)pc + oc) & 3));
      const unsigned v2 = ((const unsigned*)gw)[(lane & 15) * 20];
      asm volatile("s_barrier" ::: "memory");
      asm volatile("" :: "v"(v), "v"(v2));
      cp = ((const char*)pc + oc) - (((uintptr_t)pc + oc) & 3);
    }
    __builtin_amdgcn_s_sleep(100);
    const int t0 = (bid - 192) * 64 + lane;
    const unsigned v0 = *(const unsigned*)((const char*)wo + (size_t)t0 * 128), v1 = *(const unsigned*)((const char*)wo + (size_t)min(t0 + 4096, 6399) * 128);
    asm volatile("" :: "v"(v0), "v"(v1));
    __builtin_amdgcn_s_sleep(40);
    const unsigned v3 = __hip_atomic_load((const unsigned*)cp, __ATOMIC_RELAXED, __HIP_MEMORY_SCOPE_AGENT);
    asm volatile("" :: "v"(v3));
  }
}

constexpr int CV_CS = 4160, CV_U = 4 * CV_CS, CV_CH = CV_U + 512 + 4096, CV_OUT = 3 * CV_CH, CV_LDS = CV_OUT + 6 * 2048;

template <bool UA> DI void conv_unit(float dsk0, float dsk1, LAS char* sm0, LAS char* sm1, LAS char* ob) {
  const int lane = threadIdx.x & 63, c16 = lane & 15, g = lane >> 4;
  f32x4 acc[8];
#pragma unroll
  for (int i = 0; i < 8; ++i) acc[i] = f32x4{0.f, 0.f, 0.f, 0.f};
  const int s77 = 2047 - 16 * (15 + 112) - c16 + 8 * g;
  const int oa = (s77 & 3) * CV_CS + (s77 >> 2) * 8, ob_ = CV_U + 512 + (16 * (c16 - 15) + 8 * g) * 2;
  LAS char* pa0 = sm0 + oa; LAS char* pb0 = sm0 + ob_;
  LAS char* pa1 = sm1 + oa; LAS char* pb1 = sm1 + ob_;
  u32x4 A[2][8], B[2][8];
  auto rd = [&](int buf, int cl) {
#pragma unroll
    for (int j = 0; j < 8; ++j) {
      const int k = UA ? j : (j & 3);
      LAS char* q = (UA || j < 4 ? pa0 : pa1) + (7 - k) * 512 + (7 - cl) * 64;
      const u32x2 lo = *(volatile LAS u32x2*)(q), hi = *(volatile LAS u32x2*)(q + 8);
      A[buf][j] = u32x4{lo.x, lo.y, hi.x, hi.y};
      B[buf][j] = *(LAS u32x4*)((UA || j < 4 ? pb0 : pb1) + k * 512 + (7 - cl) * 64);
    }
  };
  rd(0, 0);
#pragma unroll
  for (int cl = 0; cl < 8; ++cl) {
    __builtin_amdgcn_sched_barrier(0);
    if (cl < 7) rd((cl + 1) & 1, cl + 1);
    if (UA) {
#pragma unroll
      for (int k = 0; k < 8; ++k)
#pragma unroll
        for (int k2 = 0; k2 + k < 8; ++k2)
          if (k + k2 >= 4) acc[k + k2] = mfma16(A[cl & 1][k], B[cl & 1][k2], acc[k + k2]);
    } else {
#pragma unroll
      for (int e = 0; e < 2; ++e)
#pragma unroll
        for (int k = 0; k < 4; ++k)
#pragma unroll
          for (int k2 = 0; k2 + k < 4; ++k2) acc[4 * e + k + k2] = mfma16(A[cl & 1][4 * e + k], B[cl & 1][4 * e + k2], acc[4 * e + k + k2]);
    }
    if (cl < 7) {
#pragma unroll
      for (int i = 0; i < (UA ? 22 : 18); ++i) { __builtin_amdgcn_sched_group_barrier(0x008, 1, 0); __builtin_amdgcn_sched_group_barrier(0x100, 1, 0); if (i < (UA ? 2 : 6)) __builtin_amdgcn_sched_group_barrier(0x100, 1, 0); }
    }
  }
  __builtin_amdgcn_sched_barrier(0);
#pragma unroll
  for (int i = UA ? 4 : 0; i < 8; ++i) {
    const f32x4 y = acc[i];
    const int tl = 16 * c16 + 4 * g;
    const float dsk = (UA || i < 4) ? dsk0 : dsk1;
    const u32x2 uv = *(LAS u32x2*)(((UA || i < 4) ? sm0 : sm1) + CV_U + 512 + (256 * (UA ? i : i & 3) + tl) * 2);
    const unsigned y01 = pk2(y[0], y[1]), y23 = pk2(y[2], y[3]);
    const unsigned s01 = pk2(lo2f(uv.x) * dsk, hi2f(uv.x) * dsk), s23 = pk2(lo2f(uv.y) * dsk, hi2f(uv.y) * dsk);
    *(LAS u32x2*)(ob + (UA ? i - 4 : i) * 512 + tl * 2) = u32x2{pk2(lo2f(y01) + lo2f(s01), hi2f(y01) + hi2f(s01)), pk2(lo2f(y23) + lo2f(s23), hi2f(y23) + hi2f(s23))};
  }
  CFENCE();
}
template <bool UA> DI void conv_flush(int d, char* ws, LAS char* ob) {
  const int lane = threadIdx.x & 63;
  const __amdgpu_buffer_rsrc_t rs = ws_rsrc(ws);
  const u32x4 o0 = *(LAS u32x4*)(ob + lane * 16), o1 = *(LAS u32x4*)(ob + 1024 + lane * 16);
  st128s(rs, (int)WS_AT + d * SL * 2 + (UA ? 2048 : 0) + lane * 16, o0);
  st128s(rs, (int)WS_AT + d * SL * 2 + (UA ? 3072 : 1024) + lane * 16, o1);
}

DI void role_conv(int bid, char* ws, LAS char* sm) {
  const int tid = threadIdx.x, w = __builtin_amdgcn_readfirstlane(tid >> 6);
  const int odd = bid & 1, c5 = 5 * (bid >> 1);
  float dsk[3];
  {
    const int m = 255 - tid;
    const int ko = w < 2 ? 1024 + (m - 1) * 4 : (w == 2 ? 1024 + (2 * m - 129) * 4 : (tid & 63) * 16);
    u32x4 kw[3], uv[3]; u32x2 k1[3] = {u32x2{0u, 0u}, u32x2{0u, 0u}, u32x2{0u, 0u}};
#pragma unroll
    for (int i = 0; i < 3; ++i) {
      const int d = c5 + (i == 2 ? 2 : 3 * odd + i);
      const char* ksr = ws + WS_KSR + (size_t)d * KSR_BYTES;
      const bool half = i == 2 && odd;
      kw[i] = *(const u32x4*)(ksr + ko);
      if (tid == 191) k1[i] = *(const u32x2*)(ksr);
      uv[i] = *(const u32x4*)(ws + WS_UT + (size_t)d * SL * 2 + (half ? tid & 127 : tid) * 16);
      dsk[i] = *(const float*)(ksr + 2052);
    }
    if (tid < 144) {
      const int i = tid / 48, r = tid - 48 * i;
      *(LAS u32x4*)(sm + i * CV_CH + (r < 32 ? CV_U + r * 16 : ((r >> 2) & 3) * CV_CS + 4096 + (r & 3) * 16)) = u32x4{0u, 0u, 0u, 0u};
    }
    __builtin_amdgcn_sched_barrier(0);
    auto mix = [](unsigned lo, unsigned hi) { return (lo & 0xffffu) | (hi & 0xffff0000u); };
#pragma unroll
    for (int i = 0; i < 3; ++i) {
      const u32x4 W = kw[i];
      u32x4 k0; u32x2 nx;
      if (w < 2) {
        k0 = u32x4{W.z, (m & 1) ? W.z : mix(W.z, W.y), W.y, W.y};
        nx = u32x2{W.y, m == 128 ? W.x : ((m & 1) ? mix(W.y, W.x) : W.y)};
      } else if (w == 2) {
        k0 = u32x4{W.w, W.z, mix(W.z, W.y), W.y};
        nx = m == 64 ? k1[i] : u32x2{W.y, W.x};
      } else {
        k0 = W;
        nx.x = (unsigned)__builtin_amdgcn_update_dpp(0, (int)W.x, 0x130, 0xf, 0xf, false);
        nx.y = (unsigned)__builtin_amdgcn_update_dpp(0, (int)W.y, 0x130, 0xf, 0xf, false);
      }
      const unsigned a0 = __builtin_amdgcn_alignbit(k0.y, k0.x, 16), a1 = __builtin_amdgcn_alignbit(k0.z, k0.y, 16),
                     a2 = __builtin_amdgcn_alignbit(k0.w, k0.z, 16), a3 = __builtin_amdgcn_alignbit(nx.x, k0.w, 16),
                     a4 = __builtin_amdgcn_alignbit(nx.y, nx.x, 16);
      LAS char* p = sm + i * CV_CH + tid * 16;
      *(LAS u32x4*)(p + 0 * CV_CS) = k0;
      *(LAS u32x4*)(p + 1 * CV_CS) = u32x4{a0, a1, a2, a3};
      *(LAS u32x4*)(p + 2 * CV_CS) = u32x4{k0.y, k0.z, k0.w, nx.x};
      *(LAS u32x4*)(p + 3 * CV_CS) = u32x4{a1, a2, a3, a4};
      *(LAS u32x4*)(p + CV_U + 512) = uv[i];
    }
  }
  bsync();
  __builtin_amdgcn_s_setprio(2);
  LAS char* ob = sm + CV_OUT + (w < 2 ? w * 2048 : (w - 1) * 4096);
  if (w < 2 || (w == 2 && !odd)) {
    conv_unit<true>(w == 0 ? dsk[0] : (w == 1 ? dsk[1] : dsk[2]), 0.f, sm + w * CV_CH, sm, ob);
    conv_flush<true>(c5 + (w == 2 ? 2 : 3 * odd + w), ws, ob);
  } else {
    const bool one = w == 2;
    conv_unit<false>(one ? dsk[2] : dsk[0], one ? dsk[2] : dsk[1], sm + (one ? 2 : 0) * CV_CH, sm + (one ? 2 : 1) * CV_CH, ob);
    conv_flush<false>(c5 + (one ? 2 : 3 * odd), ws, ob);
    if (!one) conv_flush<false>(c5 + 3 * odd + 1, ws, ob + 2048);
  }
}

constexpr int RED_BYTES = 4 * 15 * 1024;
constexpr int K3_A = RED_BYTES, K3_LDS = K3_A + 4 * 80 * 40;
constexpr int MR_P = 0, MR_A = 4 * 1680, MR_ARS = 72, MR_W = MR_A + 32 * MR_ARS, MR_Z = MR_W + 3200;
DI void role_out(int bid, const u16* __restrict__ wo, char* ws, u16* __restrict__ out, LAS char* sm) {
  const int tid = threadIdx.x, lane = tid & 63, w = __builtin_amdgcn_readfirstlane(tid >> 6);
  const int xcd = bid & 7, jj = bid >> 3, tt = 4 * xcd + (jj & 3), cc = jj >> 2;
  const int c16 = lane & 15, g = lane >> 4, q4 = c16 >> 2, p4 = lane & 3;
  const __amdgpu_buffer_rsrc_t rs = ws_rsrc(ws);
  {
    const int h = tt, hi = h & 1;
    const char* part = ws + WS_PART + (size_t)(h * 16 + 4 * w) * 1680;
    const int l1 = lane + 64 < 105 ? lane + 64 : 104;
    f32x4 pa[4], pb[4];
#pragma unroll
    for (int r = 0; r < 4; ++r) {
      pa[r] = *(const f32x4*)(part + r * 1680 + lane * 16);
      pb[r] = *(const f32x4*)(part + r * 1680 + l1 * 16);
    }
    const int ci = min(tid, 199), we = ci / 10;
    const u32x4 wv = *(const u32x4*)(wo + (size_t)(20 * h + we) * DM + 80 * cc + 8 * (ci - 10 * we));
    const int pf = (5 * h + 1) >> 1, ph = hi ? pf - 1 : pf + 2;
    const int pqb = (16 * cc + (tid >> 4)) * 20480 + c16 * 16;
    const int po0 = pqb + pf * 256, po1 = po0 + 256, po2 = pqb + ph * 256 + hi * 8;
    u32x4 pq0 = ld128s(rs, (int)WS_PQF + po0), pq1 = ld128s(rs, (int)WS_PQF + po1);
    u32x2 pq2 = __builtin_amdgcn_raw_buffer_load_b64(rs, (int)WS_PQF + po2, 0, AUX_SC1);
    if (tid < 144) *(LAS u32x4*)(sm + MR_A + tid * 16) = u32x4{0u, 0u, 0u, 0u};
    __builtin_amdgcn_sched_barrier(0);
    *(LAS f32x4*)(sm + MR_P + w * 1680 + lane * 16) = (pa[0] + pa[1]) + (pa[2] + pa[3]);
    *(LAS f32x4*)(sm + MR_P + w * 1680 + l1 * 16) = (pb[0] + pb[1]) + (pb[2] + pb[3]);
    *(LAS u32x4*)(sm + MR_W + ci * 16) = wv;
    bsync();
    if (tid < 105) {
      LAS char* p = sm + MR_P + tid * 16;
      const f32x4 s = (*(LAS f32x4*)(p) + *(LAS f32x4*)(p + 1680)) + (*(LAS f32x4*)(p + 3360) + *(LAS f32x4*)(p + 5040));
      const int e0 = tid / 5, d0 = tid - 5 * e0;
      *(LAS u32x2*)(sm + (e0 < 20 ? MR_A + e0 * MR_ARS + d0 * 8 : MR_Z + (hi ? (d0 == 0 ? 4 : d0 - 1) : d0) * 8)) = u32x2{pk2(s[0], s[1]), pk2(s[2], s[3])};
    }
    bsync();
    u32x4 A0, A1;
    {
      LAS char* p = sm + MR_A + (8 * g + q4) * MR_ARS + 8 * p4;
      const u32x2 a = trread(p), b = trread(p + 4 * MR_ARS), c = trread(p + 32), d = trread(p + 4 * MR_ARS + 32);
      A0 = u32x4{a.x, a.y, b.x, b.y}; A1 = u32x4{c.x, c.y, d.x, d.y};
    }
    const u32x4 z0 = *(LAS u32x4*)(sm + MR_Z), z1 = *(LAS u32x4*)(sm + MR_Z + 16);
    const u32x2 z2 = *(LAS u32x2*)(sm + MR_Z + 32);
    const int e0 = min(8 * g + q4, 19), e1 = min(8 * g + 4 + q4, 19);
    const int mtf = (int)WS_MTF + cc * 102400 + c16 * 16, k0 = 20 * h + 4 * g, k1 = 20 * h + 16;
    const int o0 = mtf + (k0 >> 5) * 5120 + ((k0 >> 3) & 3) * 256 + ((k0 >> 2) & 1) * 8, o1 = mtf + (k1 >> 5) * 5120 + ((k1 >> 3) & 3) * 256 + ((k1 >> 2) & 1) * 8;
    const f32x4 z4 = f32x4{0.f, 0.f, 0.f, 0.f};
#pragma unroll 1
    for (int i = w; i < 5; i += 4) {
      const u32x2 lo = trread(sm + MR_W + e0 * 160 + (20 * p4 + 4 * i) * 2), hi2 = trread(sm + MR_W + e1 * 160 + (20 * p4 + 4 * i) * 2);
      const u32x4 B = u32x4{lo.x, lo.y, hi2.x, hi2.y};
      const f32x4 D0 = mfma16(A0, B, z4), D1 = mfma16(A1, B, z4);
      st64s(rs, o0 + i * 1024, u32x2{pk2(-D0[0], -D0[1]), pk2(-D0[2], -D0[3])});
      if (g == 0) st64s(rs, o1 + i * 1024, u32x2{pk2(-D1[0], -D1[1]), pk2(-D1[2], -D1[3])});
    }
    float da = 0.f, db = 0.f;
#pragma unroll
    for (int e2 = 0; e2 < 4; ++e2) {
      da = fmaf(lo2f(pq0[e2]), lo2f(z0[e2]), da); db = fmaf(hi2f(pq0[e2]), hi2f(z0[e2]), db);
      da = fmaf(lo2f(pq1[e2]), lo2f(z1[e2]), da); db = fmaf(hi2f(pq1[e2]), hi2f(z1[e2]), db);
    }
#pragma unroll
    for (int e2 = 0; e2 < 2; ++e2) { da = fmaf(lo2f(pq2[e2]), lo2f(z2[e2]), da); db = fmaf(hi2f(pq2[e2]), hi2f(z2[e2]), db); }
    const float r = -__builtin_amdgcn_rcpf(rbf(rbf(da + db) + 1e-6f));
#pragma unroll
    for (int e2 = 0; e2 < 4; ++e2) { pq0[e2] = pk2(lo2f(pq0[e2]) * r, hi2f(pq0[e2]) * r); pq1[e2] = pk2(lo2f(pq1[e2]) * r, hi2f(pq1[e2]) * r); }
#pragma unroll
    for (int e2 = 0; e2 < 2; ++e2) pq2[e2] = pk2(lo2f(pq2[e2]) * r, hi2f(pq2[e2]) * r);
    st128s(rs, (int)WS_PQN + po0, pq0);
    st128s(rs, (int)WS_PQN + po1, pq1);
    st64s(rs, (int)WS_PQN + po2, pq2);
  }
  bsync();
  __builtin_amdgcn_sched_barrier(0);
  float gp[10];
  auto ldg = [&] {
#pragma unroll
    for (int i = 0; i < 10; ++i) gp[i] = ((const float*)(ws + WS_G))[i * SL + 64 * tt + 16 * w + c16];
  };
  const int mtf = (int)WS_MTF + ((cc * 4 + w) * 25) * 1024;
  int bo[4];
#pragma unroll
  for (int t = 0; t < 4; ++t) bo[t] = (int)WS_PQN + (((4 * tt + ((w + t) & 3)) * 4 + w) * 5) * 1024;
  u32x4 A[5][5], B[2][4];
  int sp = 0;
  ldg();
#pragma unroll 1
  do {
    CFENCE();
#pragma unroll
    for (int ks = 0; ks < 5; ++ks)
#pragma unroll
      for (int i = 0; i < 5; ++i) A[ks][i] = ld128u(rs, lane * 16 + ((ks * 5 + i) & 3) * 1024, mtf + ((ks * 5 + i) & ~3) * 1024);
    unsigned m = 0u;
#pragma unroll
    for (int ks = 0; ks < 5; ++ks)
#pragma unroll
      for (int i = 0; i < 5; ++i) m = umax4(A[ks][i], m);
    if (all_written(m)) break;
  } while (++sp < SPIN_CAP);
  const float gs = rbf((((gp[0] + gp[1]) + (gp[2] + gp[3])) + ((gp[4] + gp[5]) + (gp[6] + gp[7]))) + (gp[8] + gp[9]));
  float gt = rbf(__builtin_amdgcn_rcpf(1.f + __builtin_amdgcn_exp2f(-1.4426950408889634f * gs)));
  asm volatile("" : "+v"(gt));
  auto ld = [&](int ks) {
#pragma unroll
    for (int t = 0; t < 4; ++t) B[ks & 1][t] = ld128u(rs, lane * 16 + (ks & 3) * 1024, bo[t] + (ks & 4) * 1024);
  };
  f32x4 acc[4][5];
  unsigned sg;
#pragma unroll 1
  do {
    CFENCE();
    ld(0); ld(1);
    sg = ~0u;
#pragma unroll
    for (int t = 0; t < 4; ++t)
#pragma unroll
      for (int i = 0; i < 5; ++i) acc[t][i] = f32x4{0.f, 0.f, 0.f, 0.f};
#pragma unroll
    for (int n = 0; n < 5; ++n) {
      __builtin_amdgcn_sched_barrier(0);
#pragma unroll
      for (int t = 0; t < 4; ++t) {
        sg = and4(B[n & 1][t], sg);
#pragma unroll
        for (int i = 0; i < 5; ++i) acc[t][i] = mfma16(A[n][i], B[n & 1][t], acc[t][i]);
      }
      __builtin_amdgcn_sched_barrier(0);
      if (n + 2 < 5) ld(n + 2);
    }
  } while (!all_neg(sg) && ++sp < SPIN_CAP);
  {
    int tz = threadIdx.x; asm volatile("" : "+v"(tz));
    const int ln = tz & 63, c = tz & 15, gg = (tz >> 4) & 3;
    u32x4 av[3];
    auto ldav = [&] {
#pragma unroll
      for (int i = 0; i < 3; ++i) {
        const int ci = min(ln + 64 * i, 159);
        av[i] = ld128s(rs, (int)WS_AT + (80 * cc + (ci >> 1)) * SL * 2 + tt * 128 + w * 32 + (ci & 1) * 16);
      }
    };
    ldav();
    __builtin_amdgcn_sched_barrier(0);
    const float omg = rbf(1.f - gt);
#pragma unroll
    for (int t = 1; t < 4; ++t)
#pragma unroll
      for (int i = 0; i < 5; ++i) *(LAS f32x4*)(sm + ((((w + t) & 3) * 3 + t - 1) * 5 + i) * 1024 + ln * 16) = acc[t][i];
    bsync();
    float gb[5][4];
#pragma unroll
    for (int i = 0; i < 5; ++i) {
      f32x4 b = acc[0][i];
#pragma unroll
      for (int sl = 0; sl < 3; ++sl) b += *(LAS f32x4*)(sm + ((w * 3 + sl) * 5 + i) * 1024 + ln * 16);
      const unsigned b01 = pk2(b[0], b[1]), b23 = pk2(b[2], b[3]);
      const unsigned g01 = pk2(omg * lo2f(b01), omg * hi2f(b01)), g23 = pk2(omg * lo2f(b23), omg * hi2f(b23));
      gb[i][0] = lo2f(g01); gb[i][1] = hi2f(g01); gb[i][2] = lo2f(g23); gb[i][3] = hi2f(g23);
#pragma unroll
      for (int e = 0; e < 4; ++e) asm volatile("" : "+v"(gb[i][e]));
    }
    __builtin_amdgcn_sched_barrier(0);
#pragma unroll 1
    for (; sp < SPIN_CAP && !all_written(umax4(av[0], umax4(av[1], umax4(av[2], 0u)))); ++sp) { __builtin_amdgcn_s_sleep(4); CFENCE(); ldav(); }
    LAS char* ab = sm + K3_A + w * (80 * 40);
#pragma unroll
    for (int i = 0; i < 3; ++i) {
      const int ci = min(ln + 64 * i, 159);
      LAS char* p = ab + (ci >> 1) * 40 + (ci & 1) * 16;
      *(LAS u32x2*)p = u32x2{av[i].x, av[i].y};
      *(LAS u32x2*)(p + 8) = u32x2{av[i].z, av[i].w};
    }
    CFENCE();
    u32x2 a[5];
#pragma unroll
    for (int i = 0; i < 5; ++i) a[i] = trread(ab + (20 * gg + 4 * i + (c >> 2)) * 40 + (c & 3) * 8);
    LAS char* ob = sm + w * 15360;
#pragma unroll
    for (int i = 0; i < 5; ++i) {
      const unsigned ga01 = pk2(gt * lo2f(a[i].x), gt * hi2f(a[i].x)), ga23 = pk2(gt * lo2f(a[i].y), gt * hi2f(a[i].y));
      *(LAS u32x2*)(ob + c * 176 + (20 * gg + 4 * i) * 2) =
          u32x2{pk2(lo2f(ga01) + gb[i][0], hi2f(ga01) + gb[i][1]), pk2(lo2f(ga23) + gb[i][2], hi2f(ga23) + gb[i][3])};
    }
    CFENCE();
#pragma unroll
    for (int i = 0; i < 3; ++i) {
      const int ci = ln + 64 * i;
      if (ci < 160) {
        const int r = ci / 10, ch = ci - 10 * r;
        *(u32x4*)(out + (size_t)(64 * tt + 16 * w + r) * DM + 80 * cc + 8 * ch) = *(LAS u32x4*)(ob + r * 176 + ch * 16);
      }
    }
  }
}

constexpr int K2_LDS = K3_LDS > CV_LDS ? K3_LDS : CV_LDS;
static_assert(10 * KG_WAVE <= K1_LDS && K1_EXTRA + 4 * 4352 <= K1_LDS && NSLOT == 3, "lds");
static_assert(CV_LDS <= K2_LDS && K2_LDS <= 81920, "lds");
__global__ void __launch_bounds__(256, 2) k2(const u16* wo, char* ws, u16* out) {
  __shared__ __attribute__((aligned(16))) char smem[K2_LDS];
  LAS char* sm = (LAS char*)smem;
  const int bid = blockIdx.x;
  if (bid >= 256) role_conv(bid - 256, ws, sm);
  else role_out(bid, wo, ws, out, sm);
  asm volatile("gm_code_end:");
}

#if defined(__HIP_DEVICE_COMPILE__)
#pragma clang attribute pop
#endif
extern "C" void kernel_launch(void* const* d_in, const int* in_sizes, int n_in, void* d_out, int out_size, void* d_ws, size_t ws_size,
                              hipStream_t stream) {
  const u16* x = (const u16*)d_in[0];
  char* ws = (char*)d_ws;
  k1<<<256, 768, 0, stream>>>(x, (const u16*)d_in[1], (const u16*)d_in[2], (const u16*)d_in[3], (const u16*)d_in[4], (const u16*)d_in[5],
                                       (const u16*)d_in[6], (const u16*)d_in[7], (const u16*)d_in[8], (const u16*)d_in[10], (const u16*)d_in[11], (const u16*)d_in[12], ws);
  k2<<<512, 256, 0, stream>>>((const u16*)d_in[11], ws, (u16*)d_out);
}
```

```cpp
#include <hip/hip_runtime.h>
#include <hip/hip_bf16.h>
#include <stdint.h>

#if defined(__HIP_DEVICE_COMPILE__)
#pragma clang attribute push(__attribute__((target("no-packed-fp32-ops"))), apply_to = function)
#endif
typedef unsigned short u16;
typedef short bf16x8 __attribute__((ext_vector_type(8)));
typedef short s16x4 __attribute__((ext_vector_type(4)));
typedef float f32x2 __attribute__((ext_vector_type(2)));
typedef float f32x4 __attribute__((ext_vector_type(4)));
typedef float f32x16 __attribute__((ext_vector_type(16)));
typedef unsigned u32x2 __attribute__((ext_vector_type(2)));
typedef unsigned u32x4 __attribute__((ext_vector_type(4)));
typedef __bf16 bf16x2_t __attribute__((ext_vector_type(2)));
#define LAS __attribute__((address_space(3)))
#define DI __device__ __forceinline__

constexpr int SL = 2048, DM = 640, NS = 128;
constexpr int KSR_BYTES = 2176;

constexpr size_t WS_UT   = 0;
constexpr size_t WS_KSR  = WS_UT + (size_t)DM * SL * 2;
constexpr size_t WS_PQF  = WS_KSR + (size_t)DM * KSR_BYTES;
constexpr size_t WS_PART = WS_PQF + (size_t)SL * DM * 2;
constexpr size_t WS_G    = WS_PART + (size_t)16 * 32 * 420 * 4;
constexpr size_t WS_MTF  = WS_G + (size_t)10 * SL * 4;
constexpr size_t WS_AT   = WS_MTF + (size_t)DM * DM * 2;
constexpr size_t WS_PQN  = WS_PQF;
constexpr size_t WS_END  = WS_AT + (size_t)DM * SL * 2;
constexpr int HO_CHUNKS = (int)((WS_END - WS_MTF) / 16);

DI unsigned pk2(float a, float b) { f32x2 v = {a, b}; return __builtin_bit_cast(unsigned, __builtin_convertvector(v, bf16x2_t)); }
DI float lo2f(unsigned u) { return __builtin_bit_cast(float, u << 16); }
DI float hi2f(unsigned u) { return __builtin_bit_cast(float, u & 0xffff0000u); }
DI unsigned mv(unsigned x) { unsigned r; asm("v_mov_b32 %0, %1" : "=v"(r) : "v"(x)); return r; }
DI float rbf(float x) { return lo2f(pk2(x, x)); }
DI f32x4 mfma16(u32x4 a, u32x4 b, f32x4 c) {
  return __builtin_amdgcn_mfma_f32_16x16x32_bf16(__builtin_bit_cast(bf16x8, a), __builtin_bit_cast(bf16x8, b), c, 0, 0, 0);
}
DI u32x2 trread(LAS char* p) { return __builtin_bit_cast(u32x2, __builtin_amdgcn_ds_read_tr16_b64_v4i16((LAS s16x4*)p)); }
DI void bsync() {
  __builtin_amdgcn_fence(__ATOMIC_RELEASE, "workgroup");
  __builtin_amdgcn_s_barrier();
  __builtin_amdgcn_fence(__ATOMIC_ACQUIRE, "workgroup");
}
#define CFENCE() asm volatile("" ::: "memory")
DI float phi(float t) { return __builtin_amdgcn_fmed3f(t, 1e-6f, 3.0e38f) + __builtin_amdgcn_fmed3f(__builtin_amdgcn_exp2f(t * 1.4426950408889634f), 0.f, 1.f); }
constexpr int AUX_SC1 = 16;
DI __amdgpu_buffer_rsrc_t ws_rsrc(char* ws) { return __builtin_amdgcn_make_buffer_rsrc((void*)ws, 0, (int)WS_END, 0x00020000); }
DI u32x4 ld128s(__amdgpu_buffer_rsrc_t rs, int off) { return __builtin_amdgcn_raw_buffer_load_b128(rs, off, 0, AUX_SC1); }
DI u32x4 ld128u(__amdgpu_buffer_rsrc_t rs, int voff, int soff) { return __builtin_amdgcn_raw_buffer_load_b128(rs, voff, soff, AUX_SC1); }
DI void st128s(__amdgpu_buffer_rsrc_t rs, int off, u32x4 v) { __builtin_amdgcn_raw_buffer_store_b128(v, rs, off, 0, AUX_SC1); }
DI void st64s(__amdgpu_buffer_rsrc_t rs, int off, u32x2 v) { __builtin_amdgcn_raw_buffer_store_b64(v, rs, off, 0, AUX_SC1); }
DI void k1st128(char* ws, size_t off, u32x4 v) { __builtin_amdgcn_raw_buffer_store_b128(v, ws_rsrc(ws), (int)off, 0, AUX_SC1); }
DI void k1st64(char* ws, size_t off, u32x2 v) { __builtin_amdgcn_raw_buffer_store_b64(v, ws_rsrc(ws), (int)off, 0, AUX_SC1); }
DI unsigned umax3(unsigned a, unsigned b, unsigned c) { return max(max(a, b), c); }
DI unsigned umax4(u32x4 v, unsigned m) { return umax3(umax3(v.x, v.y, m), v.z, v.w); }
DI unsigned and4(u32x4 v, unsigned m) { return (v.x & v.y) & (v.z & v.w) & m; }
DI bool all_neg(unsigned m) { return __builtin_amdgcn_ballot_w64((int)m >= 0) == 0ull; }
DI bool all_written(unsigned m) { return __builtin_amdgcn_ballot_w64(m == 0xffffffffu) == 0ull; }
constexpr int SPIN_CAP = 1 << 17;
DI void glds16(unsigned voff, const void* sbase, unsigned lds_dst) {
  asm volatile("s_mov_b32 m0, %2\n\ts_nop 0\n\tglobal_load_lds_dwordx4 %0, %1" :: "v"(voff), "s"(sbase), "s"(lds_dst) : "memory");
}
template <int IMM> DI void gload16(u32x4& dst, unsigned voff, const void* sbase) {
  asm volatile("global_load_dwordx4 %0, %1, %2 offset:%3" : "=v"(dst) : "v"(voff), "s"(sbase), "i"(IMM) : "memory");
}
constexpr int XAREA = 16384, SLOT = XAREA + 20480, NSLOT = 4, K1_SIDE = 2 * SLOT, K1_EXTRA = K1_SIDE + XAREA, K1_CNT = NSLOT * SLOT, K1_LDS = K1_CNT + 16;
template <int C> DI float dpp_add(float v) {
  return v + __builtin_bit_cast(float, __builtin_amdgcn_update_dpp(0, __builtin_bit_cast(int, v), C, 0xf, 0xf, false));
}
DI void lbar() { asm volatile("s_barrier" ::: "memory"); }
DI void role_loader(int rt, int c0, int c1, int bs, int wr, const u16* __restrict__ x, const u16* __restrict__ wqkv, const u16* __restrict__ gw,
                    char* ws, LAS char* sm) {
  const int lane = threadIdx.x & 63;
  const unsigned smb = (unsigned)(uintptr_t)sm;
  const unsigned wdst = smb + XAREA + 640u * wr, xdst = smb + 4096u * wr;
  const unsigned xoff = (unsigned)((128 * rt + 32 * wr + (lane >> 3)) * DM + 8 * ((lane & 7) ^ (lane >> 3))) * 2u;
  unsigned woff;
  {
    const int r = lane >= 20, ch = lane - 20 * r, hf = (ch >= 10) ^ (wr >> 1);
    woff = (unsigned)(((wr >> 1) * 8 + 2 * (wr & 1) + r) * 1920 + (hf ? c1 : c0) + 8 * (ch >= 10 ? ch - 10 : ch)) * 2u;
  }
  auto issue = [&](int P, int sl, int i0, int i1) {
    const char* wbase = (const char*)wqkv + (size_t)P * 64 * 1920 * 2;
    const char* xbase = (const char*)x + P * 128;
    const unsigned so = sl * SLOT;
    if (lane < 40) {
#pragma unroll
      for (int i = 2 * i0; i < 2 * i1; ++i) glds16(woff, wbase + (4 * (i & 1) + 16 * (i >> 1)) * 1920 * 2, wdst + so + 2560u * i);
    }
#pragma unroll
    for (int i = i0; i < i1; ++i) glds16(xoff, xbase + i * 8 * DM * 2, xdst + so + 1024u * i);
  };
  issue(0, 0, 0, 4);
  issue(1, 1, 0, 2);
  asm volatile("s_waitcnt vmcnt(6)\n\ts_barrier" ::: "memory");
  issue(1, 1, 2, 4);
#pragma unroll 1
  for (int P = 2; P < 10; ++P) {
    issue(P, P & 3, 0, 4);
    asm volatile("s_waitcnt vmcnt(12)\n\ts_barrier" ::: "memory");
  }
#pragma unroll
  for (int i = 0; i < 4; ++i) glds16(xoff, (const char*)x + min(bs, 9) * 128 + i * 8 * DM * 2, xdst + K1_SIDE + 1024u * i);
  u32x4 gv;
  gload16<0>(gv, (unsigned)(64 * min(bs, 9) + 8 * (lane & 7)) * 2u, gw);
  asm volatile("s_waitcnt vmcnt(1)\n\ts_barrier\n\ts_waitcnt vmcnt(0)" : "+v"(gv) :: "memory");
  if (bs < 10) {
    const int c16 = lane & 15, g = lane >> 4, q4 = c16 >> 2, p4 = lane & 3;
    LAS char* xs = sm + K1_SIDE;
    LAS char* bb = sm + K1_EXTRA + wr * 4352;
#pragma unroll
    for (int j = 0; j < 4; ++j) {
      const int t = 32 * g + 8 * j + q4;
      const u32x2 r0 = trread(xs + t * 128 + (((2 * wr + (p4 >> 1)) ^ (t & 7)) << 4) + (p4 & 1) * 8);
      const u32x2 r1 = trread(xs + (t + 4) * 128 + (((2 * wr + (p4 >> 1)) ^ ((t + 4) & 7)) << 4) + (p4 & 1) * 8);
      *(LAS u32x4*)(bb + c16 * 272 + g * 64 + j * 16) = u32x4{r0.x, r0.y, r1.x, r1.y};
    }
    CFENCE();
#pragma unroll
    for (int i = 0; i < 4; ++i)
      k1st128(ws, WS_UT + ((size_t)(64 * bs + 16 * wr + 4 * i + g) * SL + 128 * rt) * 2 + c16 * 16, *(LAS u32x4*)(bb + (4 * i + g) * 272 + c16 * 16));
    float gsel = 0.f;
#pragma unroll
    for (int i = 0; i < 4; ++i) {
      const u32x4 xv = *(LAS u32x4*)(xs + (32 * wr + 8 * i + (lane >> 3)) * 128 + (((lane & 7) ^ (lane >> 3)) << 4));
      float v = 0.f;
#pragma unroll
      for (int e = 0; e < 4; ++e) {
        const unsigned pr = pk2(lo2f(xv[e]) * lo2f(gv[e]), hi2f(xv[e]) * hi2f(gv[e]));
        v += lo2f(pr) + hi2f(pr);
      }
      v = dpp_add<0x141>(dpp_add<0x4e>(dpp_add<0xb1>(v)));
      gsel = (lane & 7) == i ? v : gsel;
    }
    if ((lane & 7) < 4) __builtin_amdgcn_raw_buffer_store_b32(__builtin_bit_cast(unsigned, gsel), ws_rsrc(ws), (int)WS_G + (bs * SL + 128 * rt + 32 * wr + 8 * (lane & 7) + (lane >> 3)) * 4, 0, AUX_SC1);
  }
}

template <bool TRANSPOSED> DI void gemm_loop(LAS char* sm, int wr, int o0, int o1, int o2, f32x4 (&acc)[2][5]) {
  constexpr int ts = TRANSPOSED ? 32 : 40;
  const int lane = threadIdx.x & 63, c16 = lane & 15, g = lane >> 4;
  const int x0 = (32 * wr + c16) * 128 + ((g ^ (c16 & 7)) << 4), x1 = (32 * wr + c16) * 128 + (((4 + g) ^ (c16 & 7)) << 4);
  u32x4 xf[2][2], wf[2][5];
#pragma unroll
  for (int m = 0; m < 2; ++m) {
    xf[1][m] = u32x4{0u, 0u, 0u, 0u};
#pragma unroll
    for (int t = 0; t < 5; ++t) acc[m][t] = f32x4{0.f, 0.f, 0.f, 0.f};
  }
#pragma unroll
  for (int t = 0; t < 5; ++t) wf[1][t] = u32x4{0u, 0u, 0u, 0u};
  auto mm = [&](int b) {
#pragma unroll
    for (int t = 0; t < 5; ++t)
#pragma unroll
      for (int m = 0; m < 2; ++m) acc[m][t] = TRANSPOSED ? mfma16(wf[b][t], xf[b][m], acc[m][t]) : mfma16(xf[b][m], wf[b][t], acc[m][t]);
  };
  int so = 0;
#pragma unroll 1
  for (int P = 0; P < 10; ++P) {
    LAS char* sb = sm + so;
    so = so == 3 * SLOT ? 0 : so + SLOT;
    lbar();
#pragma unroll
    for (int kk = 0; kk < 2; ++kk) {
      __builtin_amdgcn_sched_barrier(0);
      LAS char* xr = sb + (kk ? x1 : x0);
      xf[kk][0] = *(LAS u32x4*)(xr); xf[kk][1] = *(LAS u32x4*)(xr + 2048);
#pragma unroll
      for (int t = 0; t < 5; ++t) {
        LAS char* p = sb + (t < 2 ? o0 + ts * t : (t < 4 ? o1 + ts * (t - 2) : o2)) + kk * 32 * 320;
        const u32x2 lo = trread(p), hi = trread(p + 8 * 320);
        wf[kk][t] = u32x4{lo.x, lo.y, hi.x, hi.y};
      }
      mm(kk ^ 1);
#pragma unroll
      for (int i = 0; i < 10; ++i) { __builtin_amdgcn_sched_group_barrier(0x008, 1, 0); __builtin_amdgcn_sched_group_barrier(0x100, 1, 0); if (i < 2) __builtin_amdgcn_sched_group_barrier(0x100, 1, 0); }
    }
    __builtin_amdgcn_sched_barrier(0);
  }
  mm(1);
}

DI void role_kv(int rt, int hq, char* ws, LAS char* sm) {
  const int tid = threadIdx.x, lane = tid & 63, w = __builtin_amdgcn_readfirstlane(tid >> 6), wr = w & 3, hb = w >> 2;
  const int c16 = lane & 15, g = lane >> 4, q4 = c16 >> 2, p4 = lane & 3;
  const int rowpart = XAREA + (16 * (g >> 1) + 4 * (g & 1) + q4) * 320 + hb * 80;
  LAS char* ro[6];
  {
    LAS char* rb = sm + 3 * SLOT + w * 4096;
    const int ea = c16 == 0 ? 20 : (q4 == 2 ? 16 + p4 : -1), eb = c16 == 0 ? 20 : (q4 == 3 ? 16 + p4 : -1);
    ro[0] = rb + (c16 * 5 + g) * 16;
    ro[1] = rb + (ea >= 0 ? ea * 5 + g : 255) * 16;
    ro[2] = rb + (eb >= 0 ? 105 + eb * 5 + g : 255) * 16;
    ro[3] = rb + (g == 0 ? c16 * 5 + 4 : 255) * 16;
    ro[4] = rb + (g == 1 ? 105 + c16 * 5 + 4 : 255) * 16;
    ro[5] = rb + (g == 0 && ea >= 0 ? ea * 5 + 4 : (g == 1 && eb >= 0 ? 105 + eb * 5 + 4 : 255)) * 16;
#pragma unroll
    for (int i = 0; i < 6; ++i) asm volatile("" : "+v"(ro[i]));
  }
  if (tid < 2) ((LAS unsigned*)(sm + K1_CNT))[tid] = 0u;
  asm volatile("s_waitcnt lgkmcnt(0)" ::: "memory");
  f32x4 acc[2][5];
  gemm_loop<false>(sm, wr, rowpart + (g & 1) * 160 + 8 * p4, rowpart + ((g & 1) ^ 1) * 160 + 8 * p4,
                   rowpart + ((g & 1) ^ (p4 >> 1)) * 160 + 32 + 40 * (p4 & 1), acc);
  u32x4 F[4], F4A, F4B;
#pragma unroll
  for (int t = 0; t < 2; ++t)
    F[t] = u32x4{pk2(phi(acc[0][t][0]), phi(acc[0][t][1])), pk2(phi(acc[0][t][2]), phi(acc[0][t][3])),
                 pk2(phi(acc[1][t][0]), phi(acc[1][t][1])), pk2(phi(acc[1][t][2]), phi(acc[1][t][3]))};
#pragma unroll
  for (int t = 2; t < 4; ++t)
    F[t] = u32x4{pk2(acc[0][t][0], acc[0][t][1]), pk2(acc[0][t][2], acc[0][t][3]), pk2(acc[1][t][0], acc[1][t][1]), pk2(acc[1][t][2], acc[1][t][3])};
  F4A = u32x4{pk2(phi(acc[0][4][0]), phi(acc[0][4][1])), pk2(phi(acc[0][4][2]), phi(acc[0][4][3])),
              pk2(phi(acc[1][4][0]), phi(acc[1][4][1])), pk2(phi(acc[1][4][2]), phi(acc[1][4][3]))};
  {
    const u32x4 v = u32x4{pk2(acc[0][4][0], acc[0][4][1]), pk2(acc[0][4][2], acc[0][4][3]), pk2(acc[1][4][0], acc[1][4][1]), pk2(acc[1][4][2], acc[1][4][3])};
    const bool one = c16 < 8;
    F4B = u32x4{one ? 0x3f803f80u : v.x, one ? 0x3f803f80u : v.y, one ? 0x3f803f80u : v.z, one ? 0x3f803f80u : v.w};
  }
  const f32x4 z4 = f32x4{0.f, 0.f, 0.f, 0.f};
  {
    const f32x4 P0 = mfma16(F[0], F[2], z4), P1 = mfma16(F[0], F4B, z4), P2 = mfma16(F4A, F[2], z4), P3 = mfma16(F4A, F4B, z4);
    const f32x4 P4 = mfma16(F[1], F[3], z4), P5 = mfma16(F[1], F4B, z4), P6 = mfma16(F4A, F[3], z4);
    *(LAS f32x4*)(ro[0]) = P0; *(LAS f32x4*)(ro[0] + 1680) = P4; *(LAS f32x4*)(ro[1]) = P1; *(LAS f32x4*)(ro[2]) = P5;
    *(LAS f32x4*)(ro[3]) = P2; *(LAS f32x4*)(ro[4]) = P6; *(LAS f32x4*)(ro[5]) = P3;
  }
  {
    LAS unsigned* cnt = (LAS unsigned*)(sm + K1_CNT) + hb;
    asm volatile("s_waitcnt lgkmcnt(0)" ::: "memory");
    if (lane == 0) __hip_atomic_fetch_add(cnt, 1u, __ATOMIC_RELAXED, __HIP_MEMORY_SCOPE_WORKGROUP);
    while (__hip_atomic_load(cnt, __ATOMIC_RELAXED, __HIP_MEMORY_SCOPE_WORKGROUP) < 4u) __builtin_amdgcn_s_sleep(1);
    CFENCE();
  }
  if ((tid & 255) < 210) {
    LAS char* rb = sm + 3 * SLOT + hb * 16384 + (tid & 255) * 16;
    const f32x4 s = (*(LAS f32x4*)(rb) + *(LAS f32x4*)(rb + 4096)) + (*(LAS f32x4*)(rb + 8192) + *(LAS f32x4*)(rb + 12288));
    const int f = tid & 255, hh = f >= 105;
    k1st128(ws, WS_PART + ((size_t)((4 * hq + 2 * hb + hh) * 16 + rt) * 105 + f - 105 * hh) * 16, __builtin_bit_cast(u32x4, s));
  }
}

DI void role_q(int rt, int qc, char* ws, LAS char* sm) {
  const int tid = threadIdx.x, lane = tid & 63, w = __builtin_amdgcn_readfirstlane(tid >> 6), wr = w & 3, ch = w >> 2;
  const int c16 = lane & 15, g = lane >> 4, q4 = c16 >> 2, p4 = lane & 3;
  const int o = XAREA + (16 * (g >> 1) + 4 * (g & 1) + q4) * 320 + (ch ^ (g & 1)) * 160 + 8 * p4;
  f32x4 acc[2][5];
  gemm_loop<true>(sm, wr, o + 32 * ch, o + 32 * ch + 64, o + (ch ? 0 : 128), acc);
  auto pair16 = [](const u32x2 x, const u32x2 y) {
    u32x4 o;
#pragma unroll
    for (int z = 0; z < 2; ++z) {
      const auto r = __builtin_amdgcn_permlane32_swap(x[z], y[z], false, false);
      const auto q = __builtin_amdgcn_permlane16_swap(r[0], r[1], false, false);
      o[z] = q[0]; o[2 + z] = q[1];
    }
    return o;
  };
  auto ph4 = [](const f32x4 a) {
    const unsigned q01 = pk2(a[0], a[1]), q23 = pk2(a[2], a[3]);
    return u32x2{pk2(phi(lo2f(q01)), phi(hi2f(q01))), pk2(phi(lo2f(q23)), phi(hi2f(q23)))};
  };
  const size_t fb = WS_PQF + (size_t)((8 * rt + 2 * wr) * 20 + 5 * qc) * 1024;
#pragma unroll
  for (int m = 0; m < 2; ++m)
#pragma unroll
    for (int u = 0; u < 2; ++u) k1st128(ws, fb + m * 20480 + (3 * ch + u) * 1024 + lane * 16, pair16(ph4(acc[m][2 * u]), ph4(acc[m][2 * u + 1])));
  k1st128(ws, fb + (g >> 1) * 20480 + 2048 + ((2 * ch + (g & 1)) * 16 + c16) * 16, pair16(ph4(acc[0][4]), ph4(acc[1][4])));
}

struct cf { float r, i; };
DI cf cmul(cf a, cf b) { return cf{a.r * b.r - a.i * b.i, a.r * b.i + a.i * b.r}; }
DI cf csq(cf a) { return cf{a.r * a.r - a.i * a.i, 2.f * a.r * a.i}; }
DI cf cexp_m(float m, float a2, float wh, float wl) {
  const float e = __builtin_amdgcn_exp2f(m * a2);
  const float ph = __builtin_amdgcn_fractf(m * wh) + m * wl;
  return cf{e * __builtin_amdgcn_cosf(ph), e * __builtin_amdgcn_sinf(ph)};
}
constexpr int KG_RS = 272, KG_Y = 21 * KG_RS, KG_T = 0, KG_WAVE = KG_Y + 32 * KG_RS;

typedef _Float16 h16x2 __attribute__((ext_vector_type(2)));
typedef _Float16 h16x8 __attribute__((ext_vector_type(8)));
DI unsigned pkh(float a, float b) { f32x2 v = {a, b}; return __builtin_bit_cast(unsigned, __builtin_convertvector(v, h16x2)); }
DI unsigned hmul(unsigned a, unsigned b) {
  unsigned t, r;
  asm("v_pk_mul_f16 %0, %1, %2 op_sel:[0,0] op_sel_hi:[0,1]" : "=v"(t) : "v"(a), "v"(b));
  asm("v_pk_fma_f16 %0, %1, %2, %3 op_sel:[1,1,0] op_sel_hi:[1,0,1] neg_lo:[0,1,0]" : "=v"(r) : "v"(a), "v"(b), "v"(t));
  return r;
}
template <int C> DI unsigned dpp_umax(unsigned v) { return max(v, (unsigned)__builtin_amdgcn_update_dpp(0, (int)v, C, 0xf, 0xf, false)); }

DI void role_kgen(int d, unsigned pAre, unsigned pAim, unsigned pBre, unsigned pBim, unsigned pCre, unsigned pCim, unsigned ldw, unsigned dskw,
                  char* ws, LAS char* sm, u32x4& oD, u32x4& oR, u32x2& oX) {
  const int lane = threadIdx.x & 63;
  const float dt = rbf(expf((d & 1) ? hi2f(ldw) : lo2f(ldw)));
  const cf W0 = cmul(cf{lo2f(pCre), lo2f(pCim)}, cf{dt * lo2f(pBre), dt * lo2f(pBim)});
  const cf W1 = cmul(cf{hi2f(pCre), hi2f(pCim)}, cf{dt * hi2f(pBre), dt * hi2f(pBim)});
  unsigned mx = max(max(__builtin_bit_cast(unsigned, W0.r) & 0x7fffffffu, __builtin_bit_cast(unsigned, W0.i) & 0x7fffffffu),
                    max(__builtin_bit_cast(unsigned, W1.r) & 0x7fffffffu, __builtin_bit_cast(unsigned, W1.i) & 0x7fffffffu));
  mx = dpp_umax<0x121>(dpp_umax<0x122>(dpp_umax<0x124>(dpp_umax<0x128>(mx))));
  const unsigned mxs = max(max((unsigned)__builtin_amdgcn_readlane((int)mx, 0), (unsigned)__builtin_amdgcn_readlane((int)mx, 16)),
                           max((unsigned)__builtin_amdgcn_readlane((int)mx, 32), (unsigned)__builtin_amdgcn_readlane((int)mx, 48)));
  const unsigned ex = min(max(mxs >> 23, 1u), 253u);
  const float wsc = __builtin_bit_cast(float, (254u - ex) << 23), wun = __builtin_bit_cast(float, ex << 23);
  const unsigned Wh0 = pkh(W0.r * wsc, W0.i * wsc), Wh1 = pkh(W1.r * wsc, W1.i * wsc);

  f32x16 acc;
#pragma unroll
  for (int i = 0; i < 16; ++i) acc[i] = 0.f;
  const int rho = lane & 31, h = lane >> 5;
  LAS char* xa = sm + (rho < 21 ? rho : 20) * KG_RS + 16 * h;
  LAS char* ya = sm + KG_Y + rho * KG_RS + 16 * h;
#pragma unroll 1
  for (int p = 0; p < 2; ++p) {
    const int sh = p ? 0 : 16;
    const float lr = dt * hi2f(pAre << sh), li = dt * hi2f(pAim << sh);
    const float a2 = lr * 1.4426950408889634f;
    const float wh = li * 0.15915494f;
    const float wl = fmaf(li, 0.15915494f, -wh) + li * 6.4206383e-9f;
    const cf E1 = cexp_m(1.f, a2, wh, wl), E8 = cexp_m(8.f, a2, wh, wl), E64 = cexp_m(64.f, a2, wh, wl), E512 = cexp_m(512.f, a2, wh, wl);
    const cf E2 = csq(E1), E4 = csq(E2), E16 = csq(E8), E32 = csq(E16), E128 = csq(E64), E256 = csq(E128), E1024 = csq(E512);
    LAS char* xw = sm + 4 * lane;
    LAS char* yw = sm + KG_Y + 4 * lane;
    auto putx = [&](int row, unsigned v) { *(LAS unsigned*)(xw + row * KG_RS) = v; };
    auto puty = [&](int row, unsigned v) { *(LAS unsigned*)(yw + row * KG_RS) = v; };
    {
      const unsigned e1 = pkh(E1.r, E1.i), e2 = pkh(E2.r, E2.i), e4 = pkh(E4.r, E4.i), e256 = pkh(E256.r, E256.i), e512 = pkh(E512.r, E512.i),
                     e1024 = pkh(E1024.r, E1024.i);
      unsigned X[8];
      X[0] = p ? Wh1 : Wh0; X[1] = hmul(X[0], e1); X[2] = hmul(X[0], e2); X[3] = hmul(X[1], e2);
#pragma unroll
      for (int i = 0; i < 4; ++i) X[4 + i] = hmul(X[i], e4);
#pragma unroll
      for (int i = 0; i < 8; ++i) putx(i, X[i]);
      const unsigned a0 = hmul(X[0], e256), a2_ = hmul(X[2], e256), a4 = hmul(X[4], e256), a6 = hmul(X[6], e256);
      putx(8, a0); putx(9, a2_); putx(10, a4); putx(11, a6);
      const unsigned b0 = hmul(X[0], e512), b4 = hmul(X[4], e512), c0 = hmul(a0, e512), c4 = hmul(a4, e512);
      putx(12, b0); putx(13, b4); putx(14, c0); putx(15, c4);
      const unsigned d0 = hmul(X[0], e1024);
      putx(16, d0); putx(17, hmul(a0, e1024)); putx(18, hmul(b0, e1024)); putx(19, hmul(c0, e1024));
      putx(20, hmul(d0, e1024));
    }
    {
      unsigned Y[16];
      const unsigned e128 = pkh(E128.r, -E128.i);
      Y[0] = 0x3c00u; Y[1] = pkh(E8.r, -E8.i); Y[2] = pkh(E16.r, -E16.i); Y[4] = pkh(E32.r, -E32.i); Y[8] = pkh(E64.r, -E64.i);
      Y[3] = hmul(Y[1], Y[2]);
#pragma unroll
      for (int i = 1; i < 4; ++i) Y[4 + i] = hmul(Y[i], Y[4]);
#pragma unroll
      for (int i = 1; i < 8; ++i) Y[8 + i] = hmul(Y[i], Y[8]);
#pragma unroll
      for (int i = 0; i < 16; ++i) puty(i, Y[i]);
      puty(16, e128);
#pragma unroll
      for (int i = 1; i < 16; ++i) puty(16 + i, hmul(Y[i], e128));
    }
    CFENCE();

#pragma unroll
    for (int ks = 0; ks < 8; ++ks) {
      const u32x4 a = *(LAS u32x4*)(xa + 32 * ks), b = *(LAS u32x4*)(ya + 32 * ks);
      acc = __builtin_amdgcn_mfma_f32_32x32x16_f16(__builtin_bit_cast(h16x8, a), __builtin_bit_cast(h16x8, b), acc, 0, 0, 0);
    }
    CFENCE();
  }
#pragma unroll
  for (int i = 0; i < 12; ++i) acc[i] *= wun;

  LAS char* T = sm + KG_T;
  const int b = rho;
  *(LAS u32x2*)(T + (8 * b + 4 * h) * 2) = u32x2{pk2(acc[0], acc[1]), pk2(acc[2], acc[3])};
  {
    const int i0 = h ? 384 + 2 * b : 256 + 4 * b, i1 = h ? 448 + 2 * b : 258 + 4 * b;
    *(LAS unsigned*)(T + i0 * 2) = pk2(acc[4], acc[5]);
    *(LAS unsigned*)(T + i1 * 2) = pk2(acc[6], acc[7]);
  }
  if (h == 0) {
#pragma unroll
    for (int a = 0; a < 4; ++a) *(LAS u16*)(T + (512 + 32 * a + b) * 2) = (u16)pk2(acc[8 + a], 0.f);
  } else if (b == 0) {
    *(LAS u16*)(T + 640 * 2) = (u16)pk2(acc[8], 0.f);
  }
  CFENCE();
  auto tb = [&](int idx) -> unsigned { return *(LAS u16*)(T + idx * 2); };
  {
    const u32x2 t = *(LAS u32x2*)(T + (384 + 4 * lane) * 2);
    oD = u32x4{(t.x & 0xffffu) * 0x10001u, (t.x >> 16) * 0x10001u, (t.y & 0xffffu) * 0x10001u, (t.y >> 16) * 0x10001u};
    oX = u32x2{tb(640) * 0x10001u, (d & 1) ? dskw & 0xffff0000u : dskw << 16};
  }
  {
    const int m = 63 - lane;
    const bool up = m >= 32;
    const int i0 = up ? 128 + 4 * m : 8 * m;
    unsigned v[8];
#pragma unroll
    for (int e = 0; e < 8; ++e) v[e] = tb(i0 + e);
    const u32x4 A = u32x4{v[4] | (v[3] << 16), v[2] * 0x10001u, v[2] | (v[1] << 16), v[0] * 0x10001u};
    const u32x4 B = u32x4{v[7] | (v[6] << 16), v[5] | (v[4] << 16), v[3] | (v[2] << 16), v[1] | (v[0] << 16)};
    oR = up ? A : B;
  }
}

__global__ void __launch_bounds__(768, 3) k1(const u16* x, const u16* Are, const u16* Aim, const u16* Bre, const u16* Bim, const u16* Cre,
                                             const u16* Cim, const u16* logdt, const u16* Dskip, const u16* wqkv, const u16* wo, const u16* gw, char* ws) {
  __shared__ __attribute__((aligned(16))) char smem[K1_LDS];
  LAS char* sm = (LAS char*)smem;
  asm volatile("gm_code_begin:");
  const int bid = blockIdx.x, xcd = bid & 7, j = bid >> 3;
  const int w = __builtin_amdgcn_readfirstlane(threadIdx.x >> 6), lane = threadIdx.x & 63;
  const bool aux = j >= 24, isq = j >= 16;
  const int rt = (xcd >> 1) * 4 + (j & 3);
  const int cq = !isq ? (xcd & 1) * 4 + (j >> 2) : (xcd & 1) * 2 + ((j >> 2) & 1);
  if (__builtin_expect(!aux && w >= 8, 1)) {
    const int bs = isq ? cq + 8 : cq;
    role_loader(rt, isq ? 160 * cq : 640 + 80 * cq, isq ? 160 * cq + 80 : 1280 + 80 * cq, bs, w - 8, x, wqkv, gw, ws, sm);
    return;
  }
  if (!aux) {
    if (!isq) role_kv(rt, cq, ws, sm);
    else role_q(rt, cq, ws, sm);
  } else if (w < 10) {
    const int d = 80 * xcd + (j - 24) * 10 + w, n2 = d * (NS / 2) + lane;
    const unsigned p0 = ((const unsigned*)Are)[n2], p1 = ((const unsigned*)Aim)[n2], p2 = ((const unsigned*)Bre)[n2], p3 = ((const unsigned*)Bim)[n2],
                   p4 = ((const unsigned*)Cre)[n2], p5 = ((const unsigned*)Cim)[n2], p6 = ((const unsigned*)logdt)[d >> 1], p7 = ((const unsigned*)Dskip)[d >> 1];
    asm volatile("s_barrier" ::: "memory");
    u32x4 oD, oR; u32x2 oX;
    role_kgen(d, p0, p1, p2, p3, p4, p5, p6, p7, ws, sm + w * KG_WAVE, oD, oR, oX);
    const size_t dst = WS_KSR + (size_t)d * KSR_BYTES;
    k1st128(ws, dst + lane * 16, oR); k1st128(ws, dst + 1024 + lane * 16, oD);
    if (lane == 0) k1st64(ws, dst + 2048, oX);
    asm volatile("s_waitcnt vmcnt(0)" ::: "memory");
    const unsigned kt = *(const unsigned*)(ws + dst + min(lane, 16) * 128);
    for (int i = d * 64 + lane; i < HO_CHUNKS; i += 640 * 64) k1st128(ws, WS_MTF + (size_t)i * 16, u32x4{~0u, ~0u, ~0u, ~0u});
    asm volatile("" :: "v"(kt));
  } else if (w == 10) {
    const char* p[3]; int step[3];
#pragma unroll
    for (int i = 0; i < 3; ++i) {
      const int pw = min((j - 24) * 3 + i, 22);
      if (pw < 8) { p[i] = (const char*)x + (size_t)((xcd >> 1) * 512 + 64 * pw + lane) * (DM * 2); step[i] = 128; }
      else {
        const int idx = (pw - 8) * 64 + lane, row = idx / 15, l = idx - 15 * row, part = l / 5;
        p[i] = (const char*)wqkv + (size_t)row * 3840 + 1280 * part + 128 * (l - 5 * part) + 640 * (xcd & 1); step[i] = 64 * 3840;
      }
    }
    unsigned v[24];
#pragma unroll
    for (int P = 2; P < 10; ++P) {
      if (P == 2) { asm volatile("s_barrier" ::: "memory"); __builtin_amdgcn_s_sleep(20); }
#pragma unroll
      for (int i = 0; i < 3; ++i) v[(P - 2) * 3 + i] = *(const unsigned*)(p[i] + (size_t)P * step[i]);
    }
#pragma unroll
    for (int i = 0; i < 24; ++i) asm volatile("" :: "v"(v[i]));
  } else {
    const char* cp;
    {
      unsigned long long pc; int lo, hi;
      asm volatile("s_getpc_b64 %0\ngm_code_ref:\n\ts_mov_b32 %1, gm_code_begin-gm_code_ref\n\ts_mov_b32 %2, gm_code_end-gm_code_ref" : "=s"(pc), "=s"(lo), "=s"(hi));
      const int o = lo + ((j - 24) * 64 + lane) * 128;
      const int oc = min(o, hi - 4);
      const unsigned v = *(const unsigned*)(((const char*)pc + oc) - (((uintptr_t)pc + oc) & 3));
      const unsigned v2 = ((const unsigned*)gw)[(lane & 15) * 20];
      const int li = min((bid - 192) * 62 + lane, 3967);
      const char* dp = li < 2048 ? (const char*)x + (size_t)li * 1280 : (const char*)wqkv + (size_t)(li - 2048) * 128;
      const int dstep = li < 2048 ? 128 : 64 * 3840;
      asm volatile("s_barrier" ::: "memory");
      unsigned dv[8];
#pragma unroll
      for (int P = 2; P < 10; ++P) dv[P - 2] = *(const unsigned*)(dp + (size_t)P * dstep);
#pragma unroll
      for (int P = 2; P < 10; ++P) asm volatile("" :: "v"(dv[P - 2]));
      asm volatile("" :: "v"(v), "v"(v2));
      cp = ((const char*)pc + oc) - (((uintptr_t)pc + oc) & 3);
    }
    __builtin_amdgcn_s_sleep(30);
    const int t0 = xcd * 800 + (j - 24) * 100 + lane;
    const unsigned v0 = *(const unsigned*)((const char*)wo + (size_t)t0 * 128), v1 = *(const unsigned*)((const char*)wo + (size_t)(t0 + 36) * 128);
    asm volatile("" :: "v"(v0), "v"(v1));
    __builtin_amdgcn_s_sleep(10);
    const unsigned v3 = __hip_atomic_load((const unsigned*)cp, __ATOMIC_RELAXED, __HIP_MEMORY_SCOPE_AGENT);
    asm volatile("" :: "v"(v3));
  }
}

constexpr int CV_CS = 4160, CV_U = 4 * CV_CS, CV_CH = CV_U + 512 + 4096, CV_OUT = 3 * CV_CH, CV_LDS = CV_OUT + 6 * 2048;

template <bool UA> DI void conv_unit(float dsk0, float dsk1, LAS char* sm0, LAS char* sm1, LAS char* ob) {
  const int lane = threadIdx.x & 63, c16 = lane & 15, g = lane >> 4;
  f32x4 acc[8];
#pragma unroll
  for (int i = 0; i < 8; ++i) acc[i] = f32x4{0.f, 0.f, 0.f, 0.f};
  const int s77 = 2047 - 16 * (15 + 112) - c16 + 8 * g;
  const int oa = (s77 & 3) * CV_CS + (s77 >> 2) * 8, ob_ = CV_U + 512 + (16 * (c16 - 15) + 8 * g) * 2;
  LAS char* pa0 = sm0 + oa; LAS char* pb0 = sm0 + ob_;
  LAS char* pa1 = sm1 + oa; LAS char* pb1 = sm1 + ob_;
  u32x4 A[2][8], B[2][8];
  auto rd = [&](int buf, int cl) {
#pragma unroll
    for (int j = 0; j < 8; ++j) {
      const int k = UA ? j : (j & 3);
      LAS char* q = (UA || j < 4 ? pa0 : pa1) + (7 - k) * 512 + (7 - cl) * 64;
      const u32x2 lo = *(volatile LAS u32x2*)(q), hi = *(volatile LAS u32x2*)(q + 8);
      A[buf][j] = u32x4{lo.x, lo.y, hi.x, hi.y};
      B[buf][j] = *(LAS u32x4*)((UA || j < 4 ? pb0 : pb1) + k * 512 + (7 - cl) * 64);
    }
  };
  rd(0, 0);
#pragma unroll
  for (int cl = 0; cl < 8; ++cl) {
    __builtin_amdgcn_sched_barrier(0);
    if (cl < 7) rd((cl + 1) & 1, cl + 1);
    if (UA) {
#pragma unroll
      for (int k = 0; k < 8; ++k)
#pragma unroll
        for (int k2 = 0; k2 + k < 8; ++k2)
          if (k + k2 >= 4) acc[k + k2] = mfma16(A[cl & 1][k], B[cl & 1][k2], acc[k + k2]);
    } else {
#pragma unroll
      for (int e = 0; e < 2; ++e)
#pragma unroll
        for (int k = 0; k < 4; ++k)
#pragma unroll
          for (int k2 = 0; k2 + k < 4; ++k2) acc[4 * e + k + k2] = mfma16(A[cl & 1][4 * e + k], B[cl & 1][4 * e + k2], acc[4 * e + k + k2]);
    }
    if (cl < 7) {
#pragma unroll
      for (int i = 0; i < (UA ? 22 : 18); ++i) { __builtin_amdgcn_sched_group_barrier(0x008, 1, 0); __builtin_amdgcn_sched_group_barrier(0x100, 1, 0); if (i < (UA ? 2 : 6)) __builtin_amdgcn_sched_group_barrier(0x100, 1, 0); }
    }
  }
  __builtin_amdgcn_sched_barrier(0);
#pragma unroll
  for (int i = UA ? 4 : 0; i < 8; ++i) {
    const f32x4 y = acc[i];
    const int tl = 16 * c16 + 4 * g;
    const float dsk = (UA || i < 4) ? dsk0 : dsk1;
    const u32x2 uv = *(LAS u32x2*)(((UA || i < 4) ? sm0 : sm1) + CV_U + 512 + (256 * (UA ? i : i & 3) + tl) * 2);
    const unsigned y01 = pk2(y[0], y[1]), y23 = pk2(y[2], y[3]);
    const unsigned s01 = pk2(lo2f(uv.x) * dsk, hi2f(uv.x) * dsk), s23 = pk2(lo2f(uv.y) * dsk, hi2f(uv.y) * dsk);
    *(LAS u32x2*)(ob + (UA ? i - 4 : i) * 512 + tl * 2) = u32x2{pk2(lo2f(y01) + lo2f(s01), hi2f(y01) + hi2f(s01)), pk2(lo2f(y23) + lo2f(s23), hi2f(y23) + hi2f(s23))};
  }
  CFENCE();
}
template <bool UA> DI void conv_flush(int d, char* ws, LAS char* ob) {
  const int lane = threadIdx.x & 63;
  const __amdgpu_buffer_rsrc_t rs = ws_rsrc(ws);
  const u32x4 o0 = *(LAS u32x4*)(ob + lane * 16), o1 = *(LAS u32x4*)(ob + 1024 + lane * 16);
  st128s(rs, (int)WS_AT + d * SL * 2 + (UA ? 2048 : 0) + lane * 16, o0);
  st128s(rs, (int)WS_AT + d * SL * 2 + (UA ? 3072 : 1024) + lane * 16, o1);
}

DI void role_conv(int bid, char* ws, LAS char* sm) {
  const int tid = threadIdx.x, w = __builtin_amdgcn_readfirstlane(tid >> 6);
  const int odd = (bid >> 3) & 1, c5 = 80 * (bid & 7) + 5 * (bid >> 4);
  float dsk[3];
  {
    const int m = 255 - tid;
    const int ko = w < 2 ? 1024 + (m - 1) * 4 : (w == 2 ? 1024 + (2 * m - 129) * 4 : (tid & 63) * 16);
    u32x4 kw[3], uv[3]; u32x2 k1[3] = {u32x2{0u, 0u}, u32x2{0u, 0u}, u32x2{0u, 0u}};
#pragma unroll
    for (int i = 0; i < 3; ++i) {
      const int d = c5 + (i == 2 ? 2 : 3 * odd + i);
      const char* ksr = ws + WS_KSR + (size_t)d * KSR_BYTES;
      const bool half = i == 2 && odd;
      kw[i] = *(const u32x4*)(ksr + ko);
      if (tid == 191) k1[i] = *(const u32x2*)(ksr);
      uv[i] = *(const u32x4*)(ws + WS_UT + (size_t)d * SL * 2 + (half ? tid & 127 : tid) * 16);
      dsk[i] = *(const float*)(ksr + 2052);
    }
    if (tid < 144) {
      const int i = tid / 48, r = tid - 48 * i;
      *(LAS u32x4*)(sm + i * CV_CH + (r < 32 ? CV_U + r * 16 : ((r >> 2) & 3) * CV_CS + 4096 + (r & 3) * 16)) = u32x4{0u, 0u, 0u, 0u};
    }
    __builtin_amdgcn_sched_barrier(0);
    auto mix = [](unsigned lo, unsigned hi) { return (lo & 0xffffu) | (hi & 0xffff0000u); };
#pragma unroll
    for (int i = 0; i < 3; ++i) {
      const u32x4 W = kw[i];
      u32x4 k0; u32x2 nx;
      if (w < 2) {
        k0 = u32x4{W.z, (m & 1) ? W.z : mix(W.z, W.y), W.y, W.y};
        nx = u32x2{W.y, m == 128 ? W.x : ((m & 1) ? mix(W.y, W.x) : W.y)};
      } else if (w == 2) {
        k0 = u32x4{mv(W.w), mv(W.z), mix(W.z, W.y), W.y};
        nx = m == 64 ? k1[i] : u32x2{W.y, W.x};
      } else {
        k0 = W;
        nx.x = (unsigned)__builtin_amdgcn_update_dpp(0, (int)W.x, 0x130, 0xf, 0xf, false);
        nx.y = (unsigned)__builtin_amdgcn_update_dpp(0, (int)W.y, 0x130, 0xf, 0xf, false);
      }
      const unsigned a0 = __builtin_amdgcn_alignbit(k0.y, k0.x, 16), a1 = __builtin_amdgcn_alignbit(k0.z, k0.y, 16),
                     a2 = __builtin_amdgcn_alignbit(k0.w, k0.z, 16), a3 = __builtin_amdgcn_alignbit(nx.x, k0.w, 16),
                     a4 = __builtin_amdgcn_alignbit(nx.y, nx.x, 16);
      LAS char* p = sm + i * CV_CH + tid * 16;
      *(LAS u32x4*)(p + 0 * CV_CS) = k0;
      *(LAS u32x4*)(p + 1 * CV_CS) = u32x4{a0, a1, a2, a3};
      *(LAS u32x4*)(p + 2 * CV_CS) = u32x4{mv(k0.y), mv(k0.z), mv(k0.w), mv(nx.x)};
      *(LAS u32x4*)(p + 3 * CV_CS) = u32x4{a1, a2, a3, a4};
      *(LAS u32x4*)(p + CV_U + 512) = uv[i];
    }
  }
  bsync();
  __builtin_amdgcn_s_setprio(2);
  LAS char* ob = sm + CV_OUT + (w < 2 ? w * 2048 : (w - 1) * 4096);
  if (w < 2 || (w == 2 && !odd)) {
    conv_unit<true>(w == 0 ? dsk[0] : (w == 1 ? dsk[1] : dsk[2]), 0.f, sm + w * CV_CH, sm, ob);
    conv_flush<true>(c5 + (w == 2 ? 2 : 3 * odd + w), ws, ob);
  } else {
    const bool one = w == 2;
    conv_unit<false>(one ? dsk[2] : dsk[0], one ? dsk[2] : dsk[1], sm + (one ? 2 : 0) * CV_CH, sm + (one ? 2 : 1) * CV_CH, ob);
    conv_flush<false>(c5 + (one ? 2 : 3 * odd), ws, ob);
    if (!one) conv_flush<false>(c5 + 3 * odd + 1, ws, ob + 2048);
  }
}

constexpr int RED_BYTES = 4 * 15 * 1024;
constexpr int K3_A = RED_BYTES, K3_LDS = K3_A + 4 * 80 * 40;
constexpr int MR_P = 0, MR_A = 4 * 1680, MR_ARS = 72, MR_W = MR_A + 32 * MR_ARS, MR_Z = MR_W + 3200;
DI void role_out(int bid, const u16* __restrict__ wo, char* ws, u16* __restrict__ out, LAS char* sm) {
  const int tid = threadIdx.x, lane = tid & 63, w = __builtin_amdgcn_readfirstlane(tid >> 6);
  const int xcd = bid & 7, jj = bid >> 3, tt = 4 * xcd + (jj & 3), cc = jj >> 2;
  const int c16 = lane & 15, g = lane >> 4, q4 = c16 >> 2, p4 = lane & 3;
  const __amdgpu_buffer_rsrc_t rs = ws_rsrc(ws);
  {
    const int h = tt, hi = h & 1;
    const char* part = ws + WS_PART + (size_t)(h * 16 + 4 * w) * 1680;
    const int l1 = lane + 64 < 105 ? lane + 64 : 104;
    f32x4 pa[4], pb[4];
#pragma unroll
    for (int r = 0; r < 4; ++r) {
      pa[r] = *(const f32x4*)(part + r * 1680 + lane * 16);
      pb[r] = *(const f32x4*)(part + r * 1680 + l1 * 16);
    }
    const int ci = min(tid, 199), we = ci / 10;
    const u32x4 wv = *(const u32x4*)(wo + (size_t)(20 * h + we) * DM + 80 * cc + 8 * (ci - 10 * we));
    const int pf = (5 * h + 1) >> 1, ph = hi ? pf - 1 : pf + 2;
    const int pqb = (16 * cc + (tid >> 4)) * 20480 + c16 * 16;
    const int po0 = pqb + pf * 256, po1 = po0 + 256, po2 = pqb + ph * 256 + hi * 8;
    u32x4 pq0 = ld128s(rs, (int)WS_PQF + po0), pq1 = ld128s(rs, (int)WS_PQF + po1);
    u32x2 pq2 = __builtin_amdgcn_raw_buffer_load_b64(rs, (int)WS_PQF + po2, 0, AUX_SC1);
    if (tid < 144) *(LAS u32x4*)(sm + MR_A + tid * 16) = u32x4{0u, 0u, 0u, 0u};
    __builtin_amdgcn_sched_barrier(0);
    *(LAS f32x4*)(sm + MR_P + w * 1680 + lane * 16) = (pa[0] + pa[1]) + (pa[2] + pa[3]);
    *(LAS f32x4*)(sm + MR_P + w * 1680 + l1 * 16) = (pb[0] + pb[1]) + (pb[2] + pb[3]);
    *(LAS u32x4*)(sm + MR_W + ci * 16) = wv;
    bsync();
    if (tid < 105) {
      LAS char* p = sm + MR_P + tid * 16;
      const f32x4 s = (*(LAS f32x4*)(p) + *(LAS f32x4*)(p + 1680)) + (*(LAS f32x4*)(p + 3360) + *(LAS f32x4*)(p + 5040));
      const int e0 = tid / 5, d0 = tid - 5 * e0;
      *(LAS u32x2*)(sm + (e0 < 20 ? MR_A + e0 * MR_ARS + d0 * 8 : MR_Z + (hi ? (d0 == 0 ? 4 : d0 - 1) : d0) * 8)) = u32x2{pk2(s[0], s[1]), pk2(s[2], s[3])};
    }
    bsync();
    u32x4 A0, A1;
    {
      LAS char* p = sm + MR_A + (8 * g + q4) * MR_ARS + 8 * p4;
      const u32x2 a = trread(p), b = trread(p + 4 * MR_ARS), c = trread(p + 32), d = trread(p + 4 * MR_ARS + 32);
      A0 = u32x4{a.x, a.y, b.x, b.y}; A1 = u32x4{c.x, c.y, d.x, d.y};
    }
    const u32x4 z0 = *(LAS u32x4*)(sm + MR_Z), z1 = *(LAS u32x4*)(sm + MR_Z + 16);
    const u32x2 z2 = *(LAS u32x2*)(sm + MR_Z + 32);
    const int e0 = min(8 * g + q4, 19), e1 = min(8 * g + 4 + q4, 19);
    const int mtf = (int)WS_MTF + cc * 102400 + c16 * 16, k0 = 20 * h + 4 * g, k1 = 20 * h + 16;
    const int o0 = mtf + (k0 >> 5) * 5120 + ((k0 >> 3) & 3) * 256 + ((k0 >> 2) & 1) * 8, o1 = mtf + (k1 >> 5) * 5120 + ((k1 >> 3) & 3) * 256 + ((k1 >> 2) & 1) * 8;
    const f32x4 z4 = f32x4{0.f, 0.f, 0.f, 0.f};
#pragma unroll 1
    for (int i = w; i < 5; i += 4) {
      const u32x2 lo = trread(sm + MR_W + e0 * 160 + (20 * p4 + 4 * i) * 2), hi2 = trread(sm + MR_W + e1 * 160 + (20 * p4 + 4 * i) * 2);
      const u32x4 B = u32x4{lo.x, lo.y, hi2.x, hi2.y};
      const f32x4 D0 = mfma16(A0, B, z4), D1 = mfma16(A1, B, z4);
      st64s(rs, o0 + i * 1024, u32x2{pk2(-D0[0], -D0[1]), pk2(-D0[2], -D0[3])});
      if (g == 0) st64s(rs, o1 + i * 1024, u32x2{pk2(-D1[0], -D1[1]), pk2(-D1[2], -D1[3])});
    }
    float da = 0.f, db = 0.f;
#pragma unroll
    for (int e2 = 0; e2 < 4; ++e2) {
      da = fmaf(lo2f(pq0[e2]), lo2f(z0[e2]), da); db = fmaf(hi2f(pq0[e2]), hi2f(z0[e2]), db);
      da = fmaf(lo2f(pq1[e2]), lo2f(z1[e2]), da); db = fmaf(hi2f(pq1[e2]), hi2f(z1[e2]), db);
    }
#pragma unroll
    for (int e2 = 0; e2 < 2; ++e2) { da = fmaf(lo2f(pq2[e2]), lo2f(z2[e2]), da); db = fmaf(hi2f(pq2[e2]), hi2f(z2[e2]), db); }
    const float r = -__builtin_amdgcn_rcpf(rbf(rbf(da + db) + 1e-6f));
#pragma unroll
    for (int e2 = 0; e2 < 4; ++e2) { pq0[e2] = pk2(lo2f(pq0[e2]) * r, hi2f(pq0[e2]) * r); pq1[e2] = pk2(lo2f(pq1[e2]) * r, hi2f(pq1[e2]) * r); }
#pragma unroll
    for (int e2 = 0; e2 < 2; ++e2) pq2[e2] = pk2(lo2f(pq2[e2]) * r, hi2f(pq2[e2]) * r);
    st128s(rs, (int)WS_PQN + po0, pq0);
    st128s(rs, (int)WS_PQN + po1, pq1);
    st64s(rs, (int)WS_PQN + po2, pq2);
  }
  bsync();
  __builtin_amdgcn_sched_barrier(0);
  float gp[10];
  auto ldg = [&] {
#pragma unroll
    for (int i = 0; i < 10; ++i) gp[i] = ((const float*)(ws + WS_G))[i * SL + 64 * tt + 16 * w + c16];
  };
  const int mtf = (int)WS_MTF + ((cc * 4 + w) * 25) * 1024;
  int bo[4];
#pragma unroll
  for (int t = 0; t < 4; ++t) bo[t] = (int)WS_PQN + (((4 * tt + ((w + t) & 3)) * 4 + w) * 5) * 1024;
  int sp = 0;
  u32x4 av[3];
  auto ldav = [&] {
#pragma unroll
    for (int i = 0; i < 3; ++i) {
      const int ci = min(lane + 64 * i, 159);
      av[i] = ld128s(rs, (int)WS_AT + (80 * cc + (ci >> 1)) * SL * 2 + tt * 128 + w * 32 + (ci & 1) * 16);
    }
  };
  f32x4 acc[5];
  u32x4 A[5][5], B[4][5];
  auto ldA = [&] {
#pragma unroll
    for (int ks = 0; ks < 5; ++ks)
#pragma unroll
      for (int i = 0; i < 5; ++i) A[ks][i] = ld128u(rs, lane * 16 + ((ks * 5 + i) & 3) * 1024, mtf + ((ks * 5 + i) & ~3) * 1024);
  };
  auto okA = [&] {
    unsigned m = 0u;
#pragma unroll
    for (int ks = 0; ks < 5; ++ks)
#pragma unroll
      for (int i = 0; i < 5; ++i) m = umax4(A[ks][i], m);
    return all_written(m);
  };
  auto ldB = [&](int t) {
#pragma unroll
    for (int ks = 0; ks < 5; ++ks) B[t][ks] = ld128u(rs, lane * 16 + (ks & 3) * 1024, bo[t] + (ks & 4) * 1024);
  };
  auto okB = [&](int t) {
    unsigned sg = ~0u;
#pragma unroll
    for (int ks = 0; ks < 5; ++ks) sg = and4(B[t][ks], sg);
    return all_neg(sg);
  };
  ldg(); ldA(); ldB(1); ldB(2);
  __builtin_amdgcn_sched_barrier(0);
  const float gs = rbf((((gp[0] + gp[1]) + (gp[2] + gp[3])) + ((gp[4] + gp[5]) + (gp[6] + gp[7]))) + (gp[8] + gp[9]));
  float gt = rbf(__builtin_amdgcn_rcpf(1.f + __builtin_amdgcn_exp2f(-1.4426950408889634f * gs)));
  asm volatile("" : "+v"(gt));
  if (!okA()) {
#pragma unroll 1
    do { CFENCE(); ldA(); } while (!okA() && ++sp < SPIN_CAP);
  }
#pragma unroll
  for (int t = 1; t < 5; ++t) {
    __builtin_amdgcn_sched_barrier(0);
    if (!okB(t & 3)) {
#pragma unroll 1
      do { CFENCE(); ldB(t & 3); } while (!okB(t & 3) && ++sp < SPIN_CAP);
    }
    if (t == 4) ldav();
    __builtin_amdgcn_sched_barrier(0);
#pragma unroll
    for (int i = 0; i < 5; ++i) acc[i] = f32x4{0.f, 0.f, 0.f, 0.f};
#pragma unroll
    for (int ks = 0; ks < 5; ++ks)
#pragma unroll
      for (int i = 0; i < 5; ++i) acc[i] = mfma16(A[ks][i], B[t & 3][ks], acc[i]);
    if (t < 4) {
#pragma unroll
      for (int i = 0; i < 5; ++i) *(LAS f32x4*)(sm + ((((w + t) & 3) * 3 + t - 1) * 5 + i) * 1024 + lane * 16) = acc[i];
    }
    __builtin_amdgcn_sched_barrier(0);
    if (t < 3) ldB((t + 2) & 3);
  }
  {
    const int ln = lane, c = c16, gg = g;
    const float omg = rbf(1.f - gt);
    bsync();
    float gb[5][4];
#pragma unroll
    for (int i = 0; i < 5; ++i) {
      f32x4 b = acc[i];
#pragma unroll
      for (int sl = 0; sl < 3; ++sl) b += *(LAS f32x4*)(sm + ((w * 3 + sl) * 5 + i) * 1024 + ln * 16);
      const unsigned b01 = pk2(b[0], b[1]), b23 = pk2(b[2], b[3]);
      const unsigned g01 = pk2(omg * lo2f(b01), omg * hi2f(b01)), g23 = pk2(omg * lo2f(b23), omg * hi2f(b23));
      gb[i][0] = lo2f(g01); gb[i][1] = hi2f(g01); gb[i][2] = lo2f(g23); gb[i][3] = hi2f(g23);
#pragma unroll
      for (int e = 0; e < 4; ++e) asm volatile("" : "+v"(gb[i][e]));
    }
    __builtin_amdgcn_sched_barrier(0);
#pragma unroll 1
    for (; sp < SPIN_CAP && !all_written(umax4(av[0], umax4(av[1], umax4(av[2], 0u)))); ++sp) { __builtin_amdgcn_s_sleep(4); CFENCE(); ldav(); }
    LAS char* ab = sm + K3_A + w * (80 * 40);
#pragma unroll
    for (int i = 0; i < 3; ++i) {
      const int ci = min(ln + 64 * i, 159);
      LAS char* p = ab + (ci >> 1) * 40 + (ci & 1) * 16;
      *(LAS u32x2*)p = u32x2{av[i].x, av[i].y};
      *(LAS u32x2*)(p + 8) = u32x2{av[i].z, av[i].w};
    }
    CFENCE();
    u32x2 a[5];
#pragma unroll
    for (int i = 0; i < 5; ++i) a[i] = trread(ab + (20 * gg + 4 * i + (c >> 2)) * 40 + (c & 3) * 8);
    LAS char* ob = sm + w * 15360;
#pragma unroll
    for (int i = 0; i < 5; ++i) {
      const unsigned ga01 = pk2(gt * lo2f(a[i].x), gt * hi2f(a[i].x)), ga23 = pk2(gt * lo2f(a[i].y), gt * hi2f(a[i].y));
      *(LAS u32x2*)(ob + c * 176 + (20 * gg + 4 * i) * 2) =
          u32x2{pk2(lo2f(ga01) + gb[i][0], hi2f(ga01) + gb[i][1]), pk2(lo2f(ga23) + gb[i][2], hi2f(ga23) + gb[i][3])};
    }
    CFENCE();
#pragma unroll
    for (int i = 0; i < 3; ++i) {
      const int ci = ln + 64 * i;
      if (ci < 160) {
        const int r = ci / 10, ch = ci - 10 * r;
        __builtin_amdgcn_raw_buffer_store_b128(*(LAS u32x4*)(ob + r * 176 + ch * 16), __builtin_amdgcn_make_buffer_rsrc((void*)out, 0, SL * DM * 2, 0x00020000), ((64 * tt + 16 * w + r) * DM + 80 * cc + 8 * ch) * 2, 0, AUX_SC1);
      }
    }
  }
}

constexpr int K2_LDS = K3_LDS > CV_LDS ? K3_LDS : CV_LDS;
static_assert(10 * KG_WAVE <= K1_LDS && K1_EXTRA + 4 * 4352 <= K1_LDS, "lds");
static_assert(CV_LDS <= K2_LDS && K2_LDS <= 81920, "lds");
__global__ void __launch_bounds__(256, 2) k2(const u16* wo, char* ws, u16* out) {
  __shared__ __attribute__((aligned(16))) char smem[K2_LDS];
  LAS char* sm = (LAS char*)smem;
  const int bid = blockIdx.x;
  if (bid >= 256) role_conv(bid - 256, ws, sm);
  else role_out(bid, wo, ws, out, sm);
  asm volatile("gm_code_end:");
}

#if defined(__HIP_DEVICE_COMPILE__)
#pragma clang attribute pop
#endif
extern "C" void kernel_launch(void* const* d_in, const int* in_sizes, int n_in, void* d_out, int out_size, void* d_ws, size_t ws_size,
                              hipStream_t stream) {
  const u16* x = (const u16*)d_in[0];
  char* ws = (char*)d_ws;
  k1<<<256, 768, 0, stream>>>(x, (const u16*)d_in[1], (const u16*)d_in[2], (const u16*)d_in[3], (const u16*)d_in[4], (const u16*)d_in[5],
                                       (const u16*)d_in[6], (const u16*)d_in[7], (const u16*)d_in[8], (const u16*)d_in[10], (const u16*)d_in[11], (const u16*)d_in[12], ws);
  k2<<<512, 256, 0, stream>>>((const u16*)d_in[11], ws, (u16*)d_out);
}
```
